```python
import torch

def _softmax(x, axis=-1):
    return torch.softmax(x, dim=axis)
def _gelu(x):
    return torch.nn.functional.gelu(x, approximate="tanh")
def _silu(x):
    return torch.nn.functional.silu(x)
def _sigmoid(x):
    return torch.sigmoid(x)
def _layernorm(x, axis=-1, eps=1e-5):
    m = x.mean(dim=axis, keepdim=True)
    v = x.var(dim=axis, keepdim=True, unbiased=False)
    return (x - m) / torch.sqrt(v + eps)
def _scatter_add(tgt, idx, src, axis=0):
    return tgt.clone().index_add_(axis, idx, src)
def _segment_sum(src, seg, n):
    out = torch.zeros((n,) + src.shape[1:], dtype=src.dtype)
    return out.index_add_(0, seg, src)
def _pad(x, pw):
    flat = tuple(v for pair in reversed(pw) for v in pair)
    return torch.nn.functional.pad(x, flat)

kernel_name = 'standalone__sliding_block_cached__decode'

def setup_inputs():

    _w = (lambda a: (lambda t: t.to(torch.bfloat16) if t.is_floating_point() else t)(torch.as_tensor(a)))
    B, Sq, D = 2, 1, 512
    Hq, Hkv, Sk, Dh = 32, 8, 32768, 128
    assert Hq % Hkv == 0, (Hq, Hkv)
    return {
        "x": _w((lambda *s: torch.randn(*s, dtype=torch.bfloat16) * 0.8)(B, Sq, D)),
        "k_cache": _w((lambda *s: torch.randn(*s, dtype=torch.bfloat16) * 0.8)(B, Hkv, Sk, Dh)),
        "v_cache": _w((lambda *s: torch.randn(*s, dtype=torch.bfloat16) * 0.05)(B, Hkv, Sk, Dh)),
        "wq": _w((lambda *s: torch.randn(*s, dtype=torch.bfloat16) * 0.8)(D, Hq * Dh) * (D ** -0.5)),
        "wo": _w((lambda *s: torch.randn(*s, dtype=torch.bfloat16) * 0.05)(Hq * Dh, D) * ((Hq * Dh) ** -0.5)),
    }

def reference(x, k_cache, v_cache, wq, wo):
    B, Sq, D = x.shape
    Hq, Dh = 32, 128
    Hkv, Sk = k_cache.shape[1], k_cache.shape[2]
    W = min(4096, max(256, Sk // 4))
    rep = Hq // Hkv
    q = (lambda x, *d: x.permute(*d))((x @ wq).reshape(B, Sq, Hq, Dh), 0, 2, 1, 3)
    k = (lambda x, n, axis: torch.repeat_interleave(x, n, dim=axis))(k_cache[:, :, Sk - W:, :], rep, axis=1)
    v = (lambda x, n, axis: torch.repeat_interleave(x, n, dim=axis))(v_cache[:, :, Sk - W:, :], rep, axis=1)
    scores = torch.einsum("bhqd,bhkd->bhqk", q, k) * (Dh ** -0.5)
    p = _softmax(scores, axis=-1)
    o = torch.einsum("bhqk,bhkd->bhqd", p, v)
    return (lambda x, *d: x.permute(*d))(o, 0, 2, 1, 3).reshape(B, Sq, Hq * Dh) @ wo
```

```cpp
#include <hip/hip_runtime.h>
#include <hip/hip_bf16.h>
#include <stdint.h>

typedef unsigned short u16;
typedef unsigned int u32;
__device__ __forceinline__ float bf2f(u16 b) { return __uint_as_float(((u32)b) << 16); }
__device__ __forceinline__ u16 f2bf(float f) { __bf16 h = (__bf16)f; return __builtin_bit_cast(u16, h); }

constexpr int B = 2, D = 512, HQ = 32, HKV = 8, SK = 32768, DH = 128, W = 4096, NS = 16;

__global__ void k_q(const u16* x, const u16* wq, float* q) {
  int n = blockIdx.x * 64 + threadIdx.x;
  float a0 = 0, a1 = 0;
  for (int k = 0; k < D; ++k) { float w = bf2f(wq[(size_t)k * 4096 + n]); a0 += bf2f(x[k]) * w; a1 += bf2f(x[512 + k]) * w; }
  q[n] = bf2f(f2bf(a0)); q[4096 + n] = bf2f(f2bf(a1));
}
__global__ void k_attn(const float* q, const u16* kc, const u16* vc, float* po, float* pml) {
  int s = blockIdx.x, h = blockIdx.y, b = blockIdx.z, kvh = h / 4, t = threadIdx.x;
  __shared__ float sq[128]; __shared__ float sp[256]; __shared__ float red[256];
  if (t < 128) sq[t] = q[b * 4096 + h * 128 + t];
  __syncthreads();
  const u16* kr = kc + ((size_t)(b * HKV + kvh) * SK + (SK - W) + s * 256 + t) * DH;
  float a = 0; for (int d = 0; d < 128; ++d) a += sq[d] * bf2f(kr[d]);
  a *= 0.08838834764831845f;
  red[t] = a; __syncthreads();
  for (int o = 128; o > 0; o >>= 1) { if (t < o) red[t] = fmaxf(red[t], red[t + o]); __syncthreads(); }
  float m = red[0]; __syncthreads();
  float p = __expf(a - m); sp[t] = p; red[t] = p; __syncthreads();
  for (int o = 128; o > 0; o >>= 1) { if (t < o) red[t] += red[t + o]; __syncthreads(); }
  float l = red[0];
  if (t < 128) {
    const u16* vr = vc + ((size_t)(b * HKV + kvh) * SK + (SK - W) + s * 256) * DH + t;
    float o = 0; for (int j = 0; j < 256; ++j) o += sp[j] * bf2f(vr[(size_t)j * DH]);
    po[((b * HQ + h) * NS + s) * 128 + t] = o;
  }
  if (t == 0) { pml[((b * HQ + h) * NS + s) * 2] = m; pml[((b * HQ + h) * NS + s) * 2 + 1] = l; }
}
__global__ void k_comb(const float* po, const float* pml, float* o) {
  int bh = blockIdx.x, t = threadIdx.x;
  float M = -INFINITY; for (int s = 0; s < NS; ++s) M = fmaxf(M, pml[(bh * NS + s) * 2]);
  float L = 0, a = 0;
  for (int s = 0; s < NS; ++s) { float w = __expf(pml[(bh * NS + s) * 2] - M); L += w * pml[(bh * NS + s) * 2 + 1]; a += w * po[(bh * NS + s) * 128 + t]; }
  o[bh * 128 + t] = bf2f(f2bf(a / L));
}
__global__ void k_o(const float* o, const u16* wo, u16* out) {
  int i = blockIdx.x * 64 + threadIdx.x; int b = i / 512, n = i % 512;
  float a = 0; for (int r = 0; r < 4096; ++r) a += o[b * 4096 + r] * bf2f(wo[(size_t)r * 512 + n]);
  out[i] = f2bf(a);
}

extern "C" void kernel_launch(void* const* d_in, const int* in_sizes, int n_in, void* d_out, int out_size, void* d_ws, size_t ws_size, hipStream_t stream) {
  const u16* x = (const u16*)d_in[0]; const u16* kc = (const u16*)d_in[1]; const u16* vc = (const u16*)d_in[2];
  const u16* wq = (const u16*)d_in[3]; const u16* wo = (const u16*)d_in[4];
  float* q = (float*)d_ws; float* po = q + 8192; float* pml = po + 1024 * 128; float* o = pml + 2048;
  k_q<<<64, 64, 0, stream>>>(x, wq, q);
  k_attn<<<dim3(NS, HQ, B), 256, 0, stream>>>(q, kc, vc, po, pml);
  k_comb<<<64, 128, 0, stream>>>(po, pml, o);
  k_o<<<16, 64, 0, stream>>>(o, wo, (u16*)d_out);
}
```

```cpp
#include <hip/hip_runtime.h>
#include <stdint.h>

typedef unsigned u32;
typedef unsigned long long u64;
typedef unsigned short u16;
typedef u32 v4u __attribute__((ext_vector_type(4)));
typedef u32 v2u __attribute__((ext_vector_type(2)));
typedef float f4 __attribute__((ext_vector_type(4)));
typedef __bf16 bf8 __attribute__((ext_vector_type(8)));
typedef __bf16 bf2 __attribute__((ext_vector_type(2)));
typedef short s4 __attribute__((ext_vector_type(4)));
#define GAS __attribute__((address_space(1)))
#define RLX_AGENT __ATOMIC_RELAXED, __HIP_MEMORY_SCOPE_AGENT
#define SB() __builtin_amdgcn_sched_barrier(0)

#define QSLEEP 12
#define WS_BASE 0

constexpr int SK = 32768, WIN = 4096;
constexpr int AUX_NT = 2, AUX_SC1 = 16;
constexpr u32 RSRC3 = 0x00020000u;
constexpr u32 OFF_Q = 4096;
constexpr u32 OFF_P2 = OFF_Q + 2 * 8 * 4 * 512 * 8;
constexpr u32 OFF_ST = OFF_P2 + 2 * 32 * 16 * 128 * 8;
constexpr u32 OFF_P3 = OFF_ST + 2 * 32 * 16 * 8;
constexpr u32 WS_BYTES = OFF_P3 + 32 * 8 * 128 * 8;
constexpr u32 SPIN_CAP = 1u << 14;
constexpr float C2 = 0.08838834764831845f * 1.4426950408889634f;

template <int CTRL> __device__ __forceinline__ float dpp(float x) {
  return __builtin_bit_cast(float, __builtin_amdgcn_mov_dpp(__builtin_bit_cast(int, x), CTRL, 0xf, 0xf, true));
}
constexpr int XOR1 = 0xB1, XOR2 = 0x4E, XOR7 = 0x141, XOR8 = 0x128;
__device__ __forceinline__ float x16_sum(float x) { auto s = __builtin_amdgcn_permlane16_swap(__float_as_uint(x), __float_as_uint(x), false, false); return __uint_as_float(s[0]) + __uint_as_float(s[1]); }
__device__ __forceinline__ float x32_sum(float x) { auto s = __builtin_amdgcn_permlane32_swap(__float_as_uint(x), __float_as_uint(x), false, false); return __uint_as_float(s[0]) + __uint_as_float(s[1]); }
__device__ __forceinline__ float x16_max(float x) { auto s = __builtin_amdgcn_permlane16_swap(__float_as_uint(x), __float_as_uint(x), false, false); return fmaxf(__uint_as_float(s[0]), __uint_as_float(s[1])); }
__device__ __forceinline__ float x32_max(float x) { auto s = __builtin_amdgcn_permlane32_swap(__float_as_uint(x), __float_as_uint(x), false, false); return fmaxf(__uint_as_float(s[0]), __uint_as_float(s[1])); }
__device__ __forceinline__ u32 pk_bf16(float lo, float hi) { bf2 v = {(__bf16)lo, (__bf16)hi}; return __builtin_bit_cast(u32, v); }
__device__ __forceinline__ float bf_lo(u32 v) { return __uint_as_float(v << 16); }
__device__ __forceinline__ float bf_hi(u32 v) { return __uint_as_float(v & 0xffff0000u); }
__device__ __forceinline__ u32 enc(float f, u32 tl) { return ((__float_as_uint(f) + 0x80u) >> 8) | tl; }
__device__ __forceinline__ float dec(u32 lo) { return __uint_as_float(lo << 8); }
__device__ __forceinline__ u32 chk(u32 lo, u32 hi, u32 tl, u32 th) { return ((lo ^ tl) & 0xff000000u) | (hi ^ th); }
__device__ __forceinline__ u32 perm_lo(u32 a, u32 b) { return __builtin_amdgcn_perm(b, a, 0x05040100u); }
__device__ __forceinline__ u32 perm_hi(u32 a, u32 b) { return __builtin_amdgcn_perm(b, a, 0x07060302u); }

#define STAMP(i)
#define STAMP_DECL
#define STAMP_FLUSH

constexpr int RED_H = 132, RED_W = 4 * RED_H;

__global__ void __launch_bounds__(1024) fused_decode(const u16* __restrict__ x, const u16* __restrict__ kc, const u16* __restrict__ vc,
                                                     const u16* __restrict__ wq, const u16* __restrict__ wo, u16* __restrict__ out,
                                                     unsigned char* __restrict__ ws
) {
  __shared__ __attribute__((aligned(16))) float lds[256 + 16 * RED_W + 128 + 16 * 128];
  u32* q_lds = (u32*)lds;
  float* red = lds + 256;
  float* stm = red + 16 * RED_W;
  float* red2 = stm + 128;
  const int bid = blockIdx.x, tid = threadIdx.x, lane = tid & 63, w = __builtin_amdgcn_readfirstlane(tid >> 6);
  const int kvh = bid & 7, r = bid >> 3, b = r & 1, s = r >> 1;
  STAMP_DECL;
  STAMP(0);
  const auto rws = __builtin_amdgcn_make_buffer_rsrc(ws, 0, WS_BYTES, RSRC3);
  const v2u ev = __builtin_amdgcn_raw_buffer_load_b64(rws, 0, 0, AUX_SC1);
  SB();
  const int l15 = lane & 15, g4 = lane >> 4;

  if (w == 0) {
    const int cb = r & 7, ks = r >> 3;
    const auto rq = __builtin_amdgcn_make_buffer_rsrc((void*)(wq + (size_t)ks * 128 * 4096 + kvh * 512 + cb * 64), 0, 128 * 8192, RSRC3);
    const auto rx = __builtin_amdgcn_make_buffer_rsrc((void*)x, 0, 2048, RSRC3);
    const int hh = (lane >> 3) & 1, c = lane & 7;
    v4u xa[2], wv[16];
    const u32 xoff = ((lane & 1) * 512 + ks * 128 + ((lane >> 1) & 1) * 32 + g4 * 8) * 2;
    xa[0] = __builtin_amdgcn_raw_buffer_load_b128(rx, xoff, 0, 0);
    xa[1] = __builtin_amdgcn_raw_buffer_load_b128(rx, xoff + 128, 0, 0);
    const u32 woff = (hh * 32 + g4 * 8) * 8192 + c * 16;
#pragma unroll
    for (int i = 0; i < 16; ++i) wv[i] = __builtin_amdgcn_raw_buffer_load_b128(rq, woff, ((i >> 3) * 64 + (i & 7)) * 8192, AUX_NT);
    SB();
    __builtin_amdgcn_s_barrier();
    SB();
    f4 acc[8];
#pragma unroll
    for (int e = 0; e < 8; ++e) acc[e] = f4{0.f, 0.f, 0.f, 0.f};
#pragma unroll
    for (int t = 0; t < 2; ++t)
#pragma unroll
      for (int e = 0; e < 8; ++e) {
        v4u bb;
#pragma unroll
        for (int m = 0; m < 4; ++m) {
          const u32 a0 = wv[8 * t + 2 * m][e >> 1], a1 = wv[8 * t + 2 * m + 1][e >> 1];
          bb[m] = (e & 1) ? perm_hi(a0, a1) : perm_lo(a0, a1);
        }
        acc[e] = __builtin_amdgcn_mfma_f32_16x16x32_bf16(__builtin_bit_cast(bf8, xa[t]), __builtin_bit_cast(bf8, bb), acc[e], 0, 0, 0);
      }
    const bool pb = lane >= 32, e1 = (lane >> 3) & 1, e2 = (lane >> 4) & 1;
    float v[8];
#pragma unroll
    for (int e = 0; e < 8; ++e) {
      const float lo = pb ? acc[e][1] : acc[e][0], hi = pb ? acc[e][3] : acc[e][2];
      const float t = hh ? hi : lo;
      v[e] = t + dpp<XOR8>(t);
    }
    const float a0 = e1 ? v[2] : v[0], a1 = e1 ? v[3] : v[1], a2 = e1 ? v[6] : v[4], a3 = e1 ? v[7] : v[5];
    const float p0 = e2 ? a2 : a0, p1 = e2 ? a3 : a1;
    v2u e0 = ev; asm volatile("" : "+v"(e0));
    const u64 T = (((u64)e0[1] << 32) | e0[0]) + 1;
    const u32 th = (u32)T, tl = ((u32)(T >> 32)) << 24;
    const v4u gr = {enc(p0, tl), th, enc(p1, tl), th};
    __builtin_amdgcn_raw_buffer_store_b128(gr, rws, OFF_Q + ((((lane >> 5) * 8 + kvh) * 4 + ks) * 512 + cb * 64 + (lane & 31) * 2) * 8, 0, AUX_SC1);
    SB();
    STAMP(1);
  } else {
    __builtin_amdgcn_s_barrier();
    SB();
  }
  const size_t kvbase = ((size_t)(b * 8 + kvh) * SK + (SK - WIN) + s * 256) * 128;
  const auto rk = __builtin_amdgcn_make_buffer_rsrc((void*)(kc + kvbase), 0, 65536, RSRC3);
  const auto rv = __builtin_amdgcn_make_buffer_rsrc((void*)(vc + kvbase), 0, 65536, RSRC3);
  v4u kf[4], vf[4];
#pragma unroll
  for (int i = 0; i < 4; ++i) kf[i] = __builtin_amdgcn_raw_buffer_load_b128(rk, l15 * 256 + g4 * 16 + i * 64, w * 4096, AUX_NT);
#pragma unroll
  for (int i = 0; i < 4; ++i) vf[i] = __builtin_amdgcn_raw_buffer_load_b128(rv, g4 * 1024 + l15 * 16 + i * 256, w * 4096, AUX_NT);
  SB();
  STAMP(2);
  v2u e1v = ev; asm volatile("" : "+v"(e1v));
  const u64 T = (((u64)e1v[1] << 32) | e1v[0]) + 1;
  const u32 th = __builtin_amdgcn_readfirstlane((u32)T), tl = __builtin_amdgcn_readfirstlane((u32)(T >> 32)) << 24;
  if (w == 0) {
    __builtin_amdgcn_s_sleep(QSLEEP);
    const u32 qoff = OFF_Q + (b * 8 + kvh) * 4 * 512 * 8 + lane * 16;
    v4u g[16];
    for (u32 spin = 0; spin < SPIN_CAP; ++spin) {
      asm volatile("" ::: "memory");
#pragma unroll
      for (int m = 0; m < 16; ++m) g[m] = __builtin_amdgcn_raw_buffer_load_b128(rws, qoff, m * 1024, AUX_SC1);
      u32 bad = 0;
#pragma unroll
      for (int m = 0; m < 16; ++m) bad |= chk(g[m][0], g[m][1], tl, th) | chk(g[m][2], g[m][3], tl, th);
      if (__all(bad == 0)) break;
      __builtin_amdgcn_s_sleep(2);
    }
    const int l5 = lane & 31, col = (lane >> 5) * 64 + (l5 & 7) * 8 + (l5 >> 3) * 2;
#pragma unroll
    for (int h = 0; h < 4; ++h) {
      const float q0 = (dec(g[h][0]) + dec(g[4 + h][0])) + (dec(g[8 + h][0]) + dec(g[12 + h][0]));
      const float q1 = (dec(g[h][2]) + dec(g[4 + h][2])) + (dec(g[8 + h][2]) + dec(g[12 + h][2]));
      q_lds[h * 64 + (col >> 1)] = pk_bf16(q0, q1);
    }
    STAMP(3);
  }
  __syncthreads();
  STAMP(4);
  const int h3 = kvh * 4 + (r >> 3), cb3 = r & 7;
  const auto ro = __builtin_amdgcn_make_buffer_rsrc((void*)(wo + (size_t)h3 * 128 * 512 + cb3 * 64), 0, 128 * 1024, RSRC3);
  const v4u wof = __builtin_amdgcn_raw_buffer_load_b128(ro, (lane & 7) * 8192 + (lane >> 3) * 16, ((w & 1) * 64 + (w >> 1)) * 1024, AUX_NT);
  SB();
  v4u qf[4];
#pragma unroll
  for (int i = 0; i < 4; ++i) qf[i] = *(const v4u*)(q_lds + (lane & 3) * 64 + i * 16 + g4 * 4);
  f4 sc = {0.f, 0.f, 0.f, 0.f};
#pragma unroll
  for (int i = 0; i < 4; ++i) sc = __builtin_amdgcn_mfma_f32_16x16x32_bf16(__builtin_bit_cast(bf8, kf[i]), __builtin_bit_cast(bf8, qf[i]), sc, 0, 0, 0);
  float mw = fmaxf(fmaxf(sc[0], sc[1]), fmaxf(sc[2], sc[3]));
  mw = x32_max(x16_max(mw)) * C2;
  const u32 p01 = pk_bf16(__builtin_amdgcn_exp2f(fmaf(sc[0], C2, -mw)), __builtin_amdgcn_exp2f(fmaf(sc[1], C2, -mw)));
  const u32 p23 = pk_bf16(__builtin_amdgcn_exp2f(fmaf(sc[2], C2, -mw)), __builtin_amdgcn_exp2f(fmaf(sc[3], C2, -mw)));
  float lw = (bf_lo(p01) + bf_hi(p01)) + (bf_lo(p23) + bf_hi(p23));
  lw = x32_sum(x16_sum(lw));
  const v2u pp = {p01, p23};
  STAMP(5);
  f4 oa[8];
#pragma unroll
  for (int e = 0; e < 8; ++e) {
    const v2u va = {(e & 1) ? perm_hi(vf[0][e >> 1], vf[1][e >> 1]) : perm_lo(vf[0][e >> 1], vf[1][e >> 1]),
                    (e & 1) ? perm_hi(vf[2][e >> 1], vf[3][e >> 1]) : perm_lo(vf[2][e >> 1], vf[3][e >> 1])};
    oa[e] = __builtin_amdgcn_mfma_f32_16x16x16bf16_1k(__builtin_bit_cast(s4, va), __builtin_bit_cast(s4, pp), f4{0.f, 0.f, 0.f, 0.f}, 0, 0, 0);
  }
  if (l15 < 4) {
#pragma unroll
    for (int e = 0; e < 8; ++e) *(f4*)(red + w * RED_W + l15 * RED_H + e * 16 + g4 * 4) = oa[e];
    if (g4 == 0) { stm[l15 * 16 + w] = mw; stm[64 + l15 * 16 + w] = lw; }
  }
  STAMP(6);
  __syncthreads();
  STAMP(7);
  {
    const int hd = w >> 2, pos = (w & 3) * 32 + (lane & 31), half = lane >> 5;
    f4 mm[4];
#pragma unroll
    for (int i = 0; i < 4; ++i) mm[i] = *(const f4*)(stm + hd * 16 + i * 4);
    const f4 l0 = *(const f4*)(stm + 64 + hd * 16 + half * 8), l1 = *(const f4*)(stm + 64 + hd * 16 + half * 8 + 4);
    float ov[8];
#pragma unroll
    for (int i = 0; i < 8; ++i) ov[i] = red[(half * 8 + i) * RED_W + hd * RED_H + pos];
    float M = fmaxf(fmaxf(fmaxf(mm[0][0], mm[0][1]), fmaxf(mm[0][2], mm[0][3])), fmaxf(fmaxf(mm[1][0], mm[1][1]), fmaxf(mm[1][2], mm[1][3])));
    M = fmaxf(M, fmaxf(fmaxf(fmaxf(mm[2][0], mm[2][1]), fmaxf(mm[2][2], mm[2][3])), fmaxf(fmaxf(mm[3][0], mm[3][1]), fmaxf(mm[3][2], mm[3][3]))));
    const f4 ma = half ? mm[2] : mm[0], mb = half ? mm[3] : mm[1];
    float O = 0.f, L = 0.f;
#pragma unroll
    for (int i = 0; i < 4; ++i) { const float a = __builtin_amdgcn_exp2f(ma[i] - M); O = fmaf(a, ov[i], O); L = fmaf(a, l0[i], L); }
#pragma unroll
    for (int i = 0; i < 4; ++i) { const float a = __builtin_amdgcn_exp2f(mb[i] - M); O = fmaf(a, ov[4 + i], O); L = fmaf(a, l1[i], L); }
    O = x32_sum(O); L = x32_sum(L);
    O *= __builtin_amdgcn_rcpf(L);
    const float On = dpp<XOR1>(O);
    const u32 rec = (b * 32 + kvh * 4 + hd) * 16 + s;
    if (lane < 32 && !(lane & 1)) {
      const v4u gr = {enc(O, tl), th, enc(On, tl), th};
      __builtin_amdgcn_raw_buffer_store_b128(gr, rws, OFF_P2 + (rec * 128 + pos) * 8, 0, AUX_SC1);
    }
    if ((w & 3) == 0 && lane == 32) {
      const v2u gr = {__float_as_uint(M + __builtin_amdgcn_logf(L)), th};
      __builtin_amdgcn_raw_buffer_store_b64(gr, rws, OFF_ST + rec * 8, 0, AUX_SC1);
    }
  }
  SB();
  __builtin_amdgcn_s_barrier();
  SB();
  STAMP(8);
  {
    const int k8 = lane >> 3, row = lane & 7;
    const u32 poff = OFF_P2 + ((h3 * 16 + 2 * k8) * 128 + w * 8 + row) * 8, soff = OFF_ST + (h3 * 16 + 2 * k8) * 8;
    v2u g[4]; v4u st[2];
    for (u32 spin = 0; spin < SPIN_CAP; ++spin) {
      asm volatile("" ::: "memory");
#pragma unroll
      for (int m = 0; m < 4; ++m) g[m] = __builtin_amdgcn_raw_buffer_load_b64(rws, poff, (m >> 1) * (32 * 16 * 128 * 8) + (m & 1) * 1024, AUX_SC1);
#pragma unroll
      for (int m = 0; m < 2; ++m) st[m] = __builtin_amdgcn_raw_buffer_load_b128(rws, soff, m * (32 * 16 * 8), AUX_SC1);
      u32 bad = (st[0][1] ^ th) | (st[0][3] ^ th) | (st[1][1] ^ th) | (st[1][3] ^ th);
#pragma unroll
      for (int m = 0; m < 4; ++m) bad |= chk(g[m][0], g[m][1], tl, th);
      if (__all(bad == 0)) break;
      __builtin_amdgcn_s_sleep(1);
    }
    STAMP(9);
    float ob[2];
#pragma unroll
    for (int bb = 0; bb < 2; ++bb) {
      const float s0 = __uint_as_float(st[bb][0]), s1 = __uint_as_float(st[bb][2]);
      float M = fmaxf(s0, s1); M = fmaxf(M, dpp<XOR8>(M)); M = x32_max(x16_max(M));
      const float w0 = __builtin_amdgcn_exp2f(s0 - M), w1 = __builtin_amdgcn_exp2f(s1 - M);
      float L = w0 + w1, O = fmaf(w0, dec(g[2 * bb][0]), w1 * dec(g[2 * bb + 1][0]));
      L += dpp<XOR8>(L); O += dpp<XOR8>(O);
      L = x32_sum(x16_sum(L)); O = x32_sum(x16_sum(O));
      ob[bb] = (float)(__bf16)(O * __builtin_amdgcn_rcpf(L));
    }
    float y[16];
#pragma unroll
    for (int i = 0; i < 4; ++i) {
      const float lo = bf_lo(wof[i]), hi = bf_hi(wof[i]);
      y[2 * i] = ob[0] * lo; y[2 * i + 1] = ob[0] * hi; y[8 + 2 * i] = ob[1] * lo; y[8 + 2 * i + 1] = ob[1] * hi;
    }
    const bool r4 = row & 4, r2 = row & 2, r1 = row & 1;
    float z[8];
#pragma unroll
    for (int i = 0; i < 8; ++i) { const float keep = r4 ? y[8 + i] : y[i], send = r4 ? y[i] : y[8 + i]; z[i] = keep + dpp<XOR7>(send); }
    float u[4];
#pragma unroll
    for (int i = 0; i < 4; ++i) { const float keep = r2 ? z[4 + i] : z[i], send = r2 ? z[i] : z[4 + i]; u[i] = keep + dpp<XOR2>(send); }
    float t2[2];
#pragma unroll
    for (int i = 0; i < 2; ++i) { const float keep = r1 ? u[2 + i] : u[i], send = r1 ? u[i] : u[2 + i]; t2[i] = keep + dpp<XOR1>(send); }
    *(float2*)(red2 + w * 128 + (row >> 2) * 64 + k8 * 8 + (row & 3) * 2) = make_float2(t2[0], t2[1]);
  }
  __syncthreads();
  STAMP(10);
  {
    const int v = w * 8 + (lane >> 3), part = lane & 7;
    float t = red2[(2 * part) * 128 + v] + red2[(2 * part + 1) * 128 + v];
    t += dpp<XOR1>(t); t += dpp<XOR2>(t); t += dpp<XOR7>(t);
    const float tn = dpp<XOR8>(t);
    if ((lane & 15) == 0) {
      const v4u gr = {enc(t, tl), th, enc(tn, tl), th};
      __builtin_amdgcn_raw_buffer_store_b128(gr, rws, OFF_P3 + ((h3 * 8 + cb3) * 128 + v) * 8, 0, AUX_SC1);
    }
  }
  SB();
  __builtin_amdgcn_s_barrier();
  SB();
  STAMP(11);
  if (w == 0) {
    const int cbf = bid >> 5, v0 = (bid & 31) * 4;
    const u32 off = OFF_P3 + (((lane >> 1) * 8 + cbf) * 128 + v0 + (lane & 1) * 2) * 8;
    v4u g;
    for (u32 spin = 0; spin < SPIN_CAP; ++spin) {
      asm volatile("" ::: "memory");
      g = __builtin_amdgcn_raw_buffer_load_b128(rws, off, 0, AUX_SC1);
      const u32 bad = chk(g[0], g[1], tl, th) | chk(g[2], g[3], tl, th);
      if (__all(bad == 0)) break;
      __builtin_amdgcn_s_sleep(1);
    }
    float a0 = dec(g[0]), a1 = dec(g[2]);
    a0 += dpp<XOR2>(a0); a1 += dpp<XOR2>(a1);
    a0 += dpp<0x124>(a0); a1 += dpp<0x124>(a1);
    a0 += dpp<XOR8>(a0); a1 += dpp<XOR8>(a1);
    a0 = x32_sum(x16_sum(a0)); a1 = x32_sum(x16_sum(a1));
    if (lane < 2) *(u32*)(out + (v0 >> 6) * 512 + cbf * 64 + (v0 & 63) + lane * 2) = pk_bf16(a0, a1);
    if (bid == 0 && lane == 0) __hip_atomic_store((GAS u64*)ws, T, RLX_AGENT);
    STAMP(12);
  }
  STAMP_FLUSH;
}

extern "C" void kernel_launch(void* const* d_in, const int* in_sizes, int n_in, void* d_out, int out_size, void* d_ws, size_t ws_size,
                              hipStream_t stream) {
  fused_decode<<<256, 1024, 0, stream>>>((const u16*)d_in[0], (const u16*)d_in[1], (const u16*)d_in[2], (const u16*)d_in[3],
                                         (const u16*)d_in[4], (u16*)d_out, (unsigned char*)d_ws + WS_BASE);
}
```

```cpp
#define AMP_I 12
#define AMP_F 0
#define AMP_BID 0
#include <hip/hip_runtime.h>
#include <stdint.h>

typedef unsigned u32;
typedef unsigned long long u64;
typedef unsigned short u16;
typedef u32 v4u __attribute__((ext_vector_type(4)));
typedef u32 v2u __attribute__((ext_vector_type(2)));
typedef float f4 __attribute__((ext_vector_type(4)));
typedef __bf16 bf8 __attribute__((ext_vector_type(8)));
typedef __bf16 bf2 __attribute__((ext_vector_type(2)));
typedef short s4 __attribute__((ext_vector_type(4)));
#define GAS __attribute__((address_space(1)))
#define RLX_AGENT __ATOMIC_RELAXED, __HIP_MEMORY_SCOPE_AGENT
#define SB() __builtin_amdgcn_sched_barrier(0)

#define QSLEEP 12
#define WS_BASE 0

constexpr int SK = 32768, WIN = 4096;
constexpr int AUX_NT = 2, AUX_SC1 = 16;
constexpr u32 RSRC3 = 0x00020000u;
constexpr u32 OFF_Q = 4096;
constexpr u32 OFF_P2 = OFF_Q + 2 * 8 * 4 * 512 * 8;
constexpr u32 OFF_ST = OFF_P2 + 2 * 32 * 16 * 128 * 8;
constexpr u32 OFF_P3 = OFF_ST + 2 * 32 * 16 * 8;
constexpr u32 WS_BYTES = OFF_P3 + 32 * 8 * 128 * 8;
constexpr u32 SPIN_CAP = 1u << 14;
constexpr float C2 = 0.08838834764831845f * 1.4426950408889634f;

template <int CTRL> __device__ __forceinline__ float dpp(float x) {
  return __builtin_bit_cast(float, __builtin_amdgcn_mov_dpp(__builtin_bit_cast(int, x), CTRL, 0xf, 0xf, true));
}
constexpr int XOR1 = 0xB1, XOR2 = 0x4E, XOR7 = 0x141, XOR8 = 0x128;
__device__ __forceinline__ float x16_sum(float x) { auto s = __builtin_amdgcn_permlane16_swap(__float_as_uint(x), __float_as_uint(x), false, false); return __uint_as_float(s[0]) + __uint_as_float(s[1]); }
__device__ __forceinline__ float x32_sum(float x) { auto s = __builtin_amdgcn_permlane32_swap(__float_as_uint(x), __float_as_uint(x), false, false); return __uint_as_float(s[0]) + __uint_as_float(s[1]); }
__device__ __forceinline__ float x16_max(float x) { auto s = __builtin_amdgcn_permlane16_swap(__float_as_uint(x), __float_as_uint(x), false, false); return fmaxf(__uint_as_float(s[0]), __uint_as_float(s[1])); }
__device__ __forceinline__ float x32_max(float x) { auto s = __builtin_amdgcn_permlane32_swap(__float_as_uint(x), __float_as_uint(x), false, false); return fmaxf(__uint_as_float(s[0]), __uint_as_float(s[1])); }
__device__ __forceinline__ u32 pk_bf16(float lo, float hi) { bf2 v = {(__bf16)lo, (__bf16)hi}; return __builtin_bit_cast(u32, v); }
__device__ __forceinline__ float bf_lo(u32 v) { return __uint_as_float(v << 16); }
__device__ __forceinline__ float bf_hi(u32 v) { return __uint_as_float(v & 0xffff0000u); }
__device__ __forceinline__ u32 enc(float f, u32 tl) { return ((__float_as_uint(f) + 0x80u) >> 8) | tl; }
__device__ __forceinline__ float dec(u32 lo) { return __uint_as_float(lo << 8); }
__device__ __forceinline__ u32 chk(u32 lo, u32 hi, u32 tl, u32 th) { return ((lo ^ tl) & 0xff000000u) | (hi ^ th); }
__device__ __forceinline__ u32 perm_lo(u32 a, u32 b) { return __builtin_amdgcn_perm(b, a, 0x05040100u); }
__device__ __forceinline__ u32 perm_hi(u32 a, u32 b) { return __builtin_amdgcn_perm(b, a, 0x07060302u); }

#define STAMP(i) do { SB(); asm volatile("s_memrealtime %0\n\ts_waitcnt lgkmcnt(0)" : "=s"(tst[i]) :: "memory"); SB(); } while (0)
#define STAMP_DECL u64 tst[13]; _Pragma("unroll") for (int i_ = 0; i_ < 13; ++i_) tst[i_] = 0
#define STAMP_FLUSH do { if (bid == AMP_BID && w == 0) { const u64 until = tst[12] + AMP_F * (tst[AMP_I] - tst[0]); for (int sp_ = 0; sp_ < 100000 && __builtin_amdgcn_s_memrealtime() < until; ++sp_) {} } } while (0)

constexpr int RED_H = 132, RED_W = 4 * RED_H;

__global__ void __launch_bounds__(1024) fused_decode(const u16* __restrict__ x, const u16* __restrict__ kc, const u16* __restrict__ vc,
                                                     const u16* __restrict__ wq, const u16* __restrict__ wo, u16* __restrict__ out,
                                                     unsigned char* __restrict__ ws
) {
  __shared__ __attribute__((aligned(16))) float lds[256 + 16 * RED_W + 128 + 16 * 128];
  u32* q_lds = (u32*)lds;
  float* red = lds + 256;
  float* stm = red + 16 * RED_W;
  float* red2 = stm + 128;
  const int bid = blockIdx.x, tid = threadIdx.x, lane = tid & 63, w = __builtin_amdgcn_readfirstlane(tid >> 6);
  const int kvh = bid & 7, r = bid >> 3, b = r & 1, s = r >> 1;
  STAMP_DECL;
  STAMP(0);
  const auto rws = __builtin_amdgcn_make_buffer_rsrc(ws, 0, WS_BYTES, RSRC3);
  const v2u ev = __builtin_amdgcn_raw_buffer_load_b64(rws, 0, 0, AUX_SC1);
  SB();
  const int l15 = lane & 15, g4 = lane >> 4;

  if (w == 0) {
    const int cb = r & 7, ks = r >> 3;
    const auto rq = __builtin_amdgcn_make_buffer_rsrc((void*)(wq + (size_t)ks * 128 * 4096 + kvh * 512 + cb * 64), 0, 128 * 8192, RSRC3);
    const auto rx = __builtin_amdgcn_make_buffer_rsrc((void*)x, 0, 2048, RSRC3);
    const int hh = (lane >> 3) & 1, c = lane & 7;
    v4u xa[2], wv[16];
    const u32 xoff = ((lane & 1) * 512 + ks * 128 + ((lane >> 1) & 1) * 32 + g4 * 8) * 2;
    xa[0] = __builtin_amdgcn_raw_buffer_load_b128(rx, xoff, 0, 0);
    xa[1] = __builtin_amdgcn_raw_buffer_load_b128(rx, xoff + 128, 0, 0);
    const u32 woff = (hh * 32 + g4 * 8) * 8192 + c * 16;
#pragma unroll
    for (int i = 0; i < 16; ++i) wv[i] = __builtin_amdgcn_raw_buffer_load_b128(rq, woff, ((i >> 3) * 64 + (i & 7)) * 8192, AUX_NT);
    SB();
    __builtin_amdgcn_s_barrier();
    SB();
    f4 acc[8];
#pragma unroll
    for (int e = 0; e < 8; ++e) acc[e] = f4{0.f, 0.f, 0.f, 0.f};
#pragma unroll
    for (int t = 0; t < 2; ++t)
#pragma unroll
      for (int e = 0; e < 8; ++e) {
        v4u bb;
#pragma unroll
        for (int m = 0; m < 4; ++m) {
          const u32 a0 = wv[8 * t + 2 * m][e >> 1], a1 = wv[8 * t + 2 * m + 1][e >> 1];
          bb[m] = (e & 1) ? perm_hi(a0, a1) : perm_lo(a0, a1);
        }
        acc[e] = __builtin_amdgcn_mfma_f32_16x16x32_bf16(__builtin_bit_cast(bf8, xa[t]), __builtin_bit_cast(bf8, bb), acc[e], 0, 0, 0);
      }
    const bool pb = lane >= 32, e1 = (lane >> 3) & 1, e2 = (lane >> 4) & 1;
    float v[8];
#pragma unroll
    for (int e = 0; e < 8; ++e) {
      const float lo = pb ? acc[e][1] : acc[e][0], hi = pb ? acc[e][3] : acc[e][2];
      const float t = hh ? hi : lo;
      v[e] = t + dpp<XOR8>(t);
    }
    const float a0 = e1 ? v[2] : v[0], a1 = e1 ? v[3] : v[1], a2 = e1 ? v[6] : v[4], a3 = e1 ? v[7] : v[5];
    const float p0 = e2 ? a2 : a0, p1 = e2 ? a3 : a1;
    v2u e0 = ev; asm volatile("" : "+v"(e0));
    const u64 T = (((u64)e0[1] << 32) | e0[0]) + 1;
    const u32 th = (u32)T, tl = ((u32)(T >> 32)) << 24;
    const v4u gr = {enc(p0, tl), th, enc(p1, tl), th};
    __builtin_amdgcn_raw_buffer_store_b128(gr, rws, OFF_Q + ((((lane >> 5) * 8 + kvh) * 4 + ks) * 512 + cb * 64 + (lane & 31) * 2) * 8, 0, AUX_SC1);
    SB();
    STAMP(1);
  } else {
    __builtin_amdgcn_s_barrier();
    SB();
  }
  const size_t kvbase = ((size_t)(b * 8 + kvh) * SK + (SK - WIN) + s * 256) * 128;
  const auto rk = __builtin_amdgcn_make_buffer_rsrc((void*)(kc + kvbase), 0, 65536, RSRC3);
  const auto rv = __builtin_amdgcn_make_buffer_rsrc((void*)(vc + kvbase), 0, 65536, RSRC3);
  v4u kf[4], vf[4];
#pragma unroll
  for (int i = 0; i < 4; ++i) kf[i] = __builtin_amdgcn_raw_buffer_load_b128(rk, l15 * 256 + g4 * 16 + i * 64, w * 4096, AUX_NT);
#pragma unroll
  for (int i = 0; i < 4; ++i) vf[i] = __builtin_amdgcn_raw_buffer_load_b128(rv, g4 * 1024 + l15 * 16 + i * 256, w * 4096, AUX_NT);
  SB();
  STAMP(2);
  v2u e1v = ev; asm volatile("" : "+v"(e1v));
  const u64 T = (((u64)e1v[1] << 32) | e1v[0]) + 1;
  const u32 th = __builtin_amdgcn_readfirstlane((u32)T), tl = __builtin_amdgcn_readfirstlane((u32)(T >> 32)) << 24;
  if (w == 0) {
    __builtin_amdgcn_s_sleep(QSLEEP);
    const u32 qoff = OFF_Q + (b * 8 + kvh) * 4 * 512 * 8 + lane * 16;
    v4u g[16];
    for (u32 spin = 0; spin < SPIN_CAP; ++spin) {
      asm volatile("" ::: "memory");
#pragma unroll
      for (int m = 0; m < 16; ++m) g[m] = __builtin_amdgcn_raw_buffer_load_b128(rws, qoff, m * 1024, AUX_SC1);
      u32 bad = 0;
#pragma unroll
      for (int m = 0; m < 16; ++m) bad |= chk(g[m][0], g[m][1], tl, th) | chk(g[m][2], g[m][3], tl, th);
      if (__all(bad == 0)) break;
      __builtin_amdgcn_s_sleep(2);
    }
    const int l5 = lane & 31, col = (lane >> 5) * 64 + (l5 & 7) * 8 + (l5 >> 3) * 2;
#pragma unroll
    for (int h = 0; h < 4; ++h) {
      const float q0 = (dec(g[h][0]) + dec(g[4 + h][0])) + (dec(g[8 + h][0]) + dec(g[12 + h][0]));
      const float q1 = (dec(g[h][2]) + dec(g[4 + h][2])) + (dec(g[8 + h][2]) + dec(g[12 + h][2]));
      q_lds[h * 64 + (col >> 1)] = pk_bf16(q0, q1);
    }
    STAMP(3);
  }
  __syncthreads();
  STAMP(4);
  const int h3 = kvh * 4 + (r >> 3), cb3 = r & 7;
  const auto ro = __builtin_amdgcn_make_buffer_rsrc((void*)(wo + (size_t)h3 * 128 * 512 + cb3 * 64), 0, 128 * 1024, RSRC3);
  const v4u wof = __builtin_amdgcn_raw_buffer_load_b128(ro, (lane & 7) * 8192 + (lane >> 3) * 16, ((w & 1) * 64 + (w >> 1)) * 1024, AUX_NT);
  SB();
  v4u qf[4];
#pragma unroll
  for (int i = 0; i < 4; ++i) qf[i] = *(const v4u*)(q_lds + (lane & 3) * 64 + i * 16 + g4 * 4);
  f4 sc = {0.f, 0.f, 0.f, 0.f};
#pragma unroll
  for (int i = 0; i < 4; ++i) sc = __builtin_amdgcn_mfma_f32_16x16x32_bf16(__builtin_bit_cast(bf8, kf[i]), __builtin_bit_cast(bf8, qf[i]), sc, 0, 0, 0);
  float mw = fmaxf(fmaxf(sc[0], sc[1]), fmaxf(sc[2], sc[3]));
  mw = x32_max(x16_max(mw)) * C2;
  const u32 p01 = pk_bf16(__builtin_amdgcn_exp2f(fmaf(sc[0], C2, -mw)), __builtin_amdgcn_exp2f(fmaf(sc[1], C2, -mw)));
  const u32 p23 = pk_bf16(__builtin_amdgcn_exp2f(fmaf(sc[2], C2, -mw)), __builtin_amdgcn_exp2f(fmaf(sc[3], C2, -mw)));
  float lw = (bf_lo(p01) + bf_hi(p01)) + (bf_lo(p23) + bf_hi(p23));
  lw = x32_sum(x16_sum(lw));
  const v2u pp = {p01, p23};
  STAMP(5);
  f4 oa[8];
#pragma unroll
  for (int e = 0; e < 8; ++e) {
    const v2u va = {(e & 1) ? perm_hi(vf[0][e >> 1], vf[1][e >> 1]) : perm_lo(vf[0][e >> 1], vf[1][e >> 1]),
                    (e & 1) ? perm_hi(vf[2][e >> 1], vf[3][e >> 1]) : perm_lo(vf[2][e >> 1], vf[3][e >> 1])};
    oa[e] = __builtin_amdgcn_mfma_f32_16x16x16bf16_1k(__builtin_bit_cast(s4, va), __builtin_bit_cast(s4, pp), f4{0.f, 0.f, 0.f, 0.f}, 0, 0, 0);
  }
  if (l15 < 4) {
#pragma unroll
    for (int e = 0; e < 8; ++e) *(f4*)(red + w * RED_W + l15 * RED_H + e * 16 + g4 * 4) = oa[e];
    if (g4 == 0) { stm[l15 * 16 + w] = mw; stm[64 + l15 * 16 + w] = lw; }
  }
  STAMP(6);
  __syncthreads();
  STAMP(7);
  {
    const int hd = w >> 2, pos = (w & 3) * 32 + (lane & 31), half = lane >> 5;
    f4 mm[4];
#pragma unroll
    for (int i = 0; i < 4; ++i) mm[i] = *(const f4*)(stm + hd * 16 + i * 4);
    const f4 l0 = *(const f4*)(stm + 64 + hd * 16 + half * 8), l1 = *(const f4*)(stm + 64 + hd * 16 + half * 8 + 4);
    float ov[8];
#pragma unroll
    for (int i = 0; i < 8; ++i) ov[i] = red[(half * 8 + i) * RED_W + hd * RED_H + pos];
    float M = fmaxf(fmaxf(fmaxf(mm[0][0], mm[0][1]), fmaxf(mm[0][2], mm[0][3])), fmaxf(fmaxf(mm[1][0], mm[1][1]), fmaxf(mm[1][2], mm[1][3])));
    M = fmaxf(M, fmaxf(fmaxf(fmaxf(mm[2][0], mm[2][1]), fmaxf(mm[2][2], mm[2][3])), fmaxf(fmaxf(mm[3][0], mm[3][1]), fmaxf(mm[3][2], mm[3][3]))));
    const f4 ma = half ? mm[2] : mm[0], mb = half ? mm[3] : mm[1];
    float O = 0.f, L = 0.f;
#pragma unroll
    for (int i = 0; i < 4; ++i) { const float a = __builtin_amdgcn_exp2f(ma[i] - M); O = fmaf(a, ov[i], O); L = fmaf(a, l0[i], L); }
#pragma unroll
    for (int i = 0; i < 4; ++i) { const float a = __builtin_amdgcn_exp2f(mb[i] - M); O = fmaf(a, ov[4 + i], O); L = fmaf(a, l1[i], L); }
    O = x32_sum(O); L = x32_sum(L);
    O *= __builtin_amdgcn_rcpf(L);
    const float On = dpp<XOR1>(O);
    const u32 rec = (b * 32 + kvh * 4 + hd) * 16 + s;
    if (lane < 32 && !(lane & 1)) {
      const v4u gr = {enc(O, tl), th, enc(On, tl), th};
      __builtin_amdgcn_raw_buffer_store_b128(gr, rws, OFF_P2 + (rec * 128 + pos) * 8, 0, AUX_SC1);
    }
    if ((w & 3) == 0 && lane == 32) {
      const v2u gr = {__float_as_uint(M + __builtin_amdgcn_logf(L)), th};
      __builtin_amdgcn_raw_buffer_store_b64(gr, rws, OFF_ST + rec * 8, 0, AUX_SC1);
    }
  }
  SB();
  __builtin_amdgcn_s_barrier();
  SB();
  STAMP(8);
  {
    const int k8 = lane >> 3, row = lane & 7;
    const u32 poff = OFF_P2 + ((h3 * 16 + 2 * k8) * 128 + w * 8 + row) * 8, soff = OFF_ST + (h3 * 16 + 2 * k8) * 8;
    v2u g[4]; v4u st[2];
    for (u32 spin = 0; spin < SPIN_CAP; ++spin) {
      asm volatile("" ::: "memory");
#pragma unroll
      for (int m = 0; m < 4; ++m) g[m] = __builtin_amdgcn_raw_buffer_load_b64(rws, poff, (m >> 1) * (32 * 16 * 128 * 8) + (m & 1) * 1024, AUX_SC1);
#pragma unroll
      for (int m = 0; m < 2; ++m) st[m] = __builtin_amdgcn_raw_buffer_load_b128(rws, soff, m * (32 * 16 * 8), AUX_SC1);
      u32 bad = (st[0][1] ^ th) | (st[0][3] ^ th) | (st[1][1] ^ th) | (st[1][3] ^ th);
#pragma unroll
      for (int m = 0; m < 4; ++m) bad |= chk(g[m][0], g[m][1], tl, th);
      if (__all(bad == 0)) break;
      __builtin_amdgcn_s_sleep(1);
    }
    STAMP(9);
    float ob[2];
#pragma unroll
    for (int bb = 0; bb < 2; ++bb) {
      const float s0 = __uint_as_float(st[bb][0]), s1 = __uint_as_float(st[bb][2]);
      float M = fmaxf(s0, s1); M = fmaxf(M, dpp<XOR8>(M)); M = x32_max(x16_max(M));
      const float w0 = __builtin_amdgcn_exp2f(s0 - M), w1 = __builtin_amdgcn_exp2f(s1 - M);
      float L = w0 + w1, O = fmaf(w0, dec(g[2 * bb][0]), w1 * dec(g[2 * bb + 1][0]));
      L += dpp<XOR8>(L); O += dpp<XOR8>(O);
      L = x32_sum(x16_sum(L)); O = x32_sum(x16_sum(O));
      ob[bb] = (float)(__bf16)(O * __builtin_amdgcn_rcpf(L));
    }
    float y[16];
#pragma unroll
    for (int i = 0; i < 4; ++i) {
      const float lo = bf_lo(wof[i]), hi = bf_hi(wof[i]);
      y[2 * i] = ob[0] * lo; y[2 * i + 1] = ob[0] * hi; y[8 + 2 * i] = ob[1] * lo; y[8 + 2 * i + 1] = ob[1] * hi;
    }
    const bool r4 = row & 4, r2 = row & 2, r1 = row & 1;
    float z[8];
#pragma unroll
    for (int i = 0; i < 8; ++i) { const float keep = r4 ? y[8 + i] : y[i], send = r4 ? y[i] : y[8 + i]; z[i] = keep + dpp<XOR7>(send); }
    float u[4];
#pragma unroll
    for (int i = 0; i < 4; ++i) { const float keep = r2 ? z[4 + i] : z[i], send = r2 ? z[i] : z[4 + i]; u[i] = keep + dpp<XOR2>(send); }
    float t2[2];
#pragma unroll
    for (int i = 0; i < 2; ++i) { const float keep = r1 ? u[2 + i] : u[i], send = r1 ? u[i] : u[2 + i]; t2[i] = keep + dpp<XOR1>(send); }
    *(float2*)(red2 + w * 128 + (row >> 2) * 64 + k8 * 8 + (row & 3) * 2) = make_float2(t2[0], t2[1]);
  }
  __syncthreads();
  STAMP(10);
  {
    const int v = w * 8 + (lane >> 3), part = lane & 7;
    float t = red2[(2 * part) * 128 + v] + red2[(2 * part + 1) * 128 + v];
    t += dpp<XOR1>(t); t += dpp<XOR2>(t); t += dpp<XOR7>(t);
    const float tn = dpp<XOR8>(t);
    if ((lane & 15) == 0) {
      const v4u gr = {enc(t, tl), th, enc(tn, tl), th};
      __builtin_amdgcn_raw_buffer_store_b128(gr, rws, OFF_P3 + ((h3 * 8 + cb3) * 128 + v) * 8, 0, AUX_SC1);
    }
  }
  SB();
  __builtin_amdgcn_s_barrier();
  SB();
  STAMP(11);
  if (w == 0) {
    const int cbf = bid >> 5, v0 = (bid & 31) * 4;
    const u32 off = OFF_P3 + (((lane >> 1) * 8 + cbf) * 128 + v0 + (lane & 1) * 2) * 8;
    v4u g;
    for (u32 spin = 0; spin < SPIN_CAP; ++spin) {
      asm volatile("" ::: "memory");
      g = __builtin_amdgcn_raw_buffer_load_b128(rws, off, 0, AUX_SC1);
      const u32 bad = chk(g[0], g[1], tl, th) | chk(g[2], g[3], tl, th);
      if (__all(bad == 0)) break;
      __builtin_amdgcn_s_sleep(1);
    }
    float a0 = dec(g[0]), a1 = dec(g[2]);
    a0 += dpp<XOR2>(a0); a1 += dpp<XOR2>(a1);
    a0 += dpp<0x124>(a0); a1 += dpp<0x124>(a1);
    a0 += dpp<XOR8>(a0); a1 += dpp<XOR8>(a1);
    a0 = x32_sum(x16_sum(a0)); a1 = x32_sum(x16_sum(a1));
    if (lane < 2) *(u32*)(out + (v0 >> 6) * 512 + cbf * 64 + (v0 & 63) + lane * 2) = pk_bf16(a0, a1);
    if (bid == 0 && lane == 0) __hip_atomic_store((GAS u64*)ws, T, RLX_AGENT);
    STAMP(12);
  }
  STAMP_FLUSH;
}

extern "C" void kernel_launch(void* const* d_in, const int* in_sizes, int n_in, void* d_out, int out_size, void* d_ws, size_t ws_size,
                              hipStream_t stream) {
  fused_decode<<<256, 1024, 0, stream>>>((const u16*)d_in[0], (const u16*)d_in[1], (const u16*)d_in[2], (const u16*)d_in[3],
                                         (const u16*)d_in[4], (u16*)d_out, (unsigned char*)d_ws + WS_BASE);
}
```

```cpp
#define AMP_I 12
#define AMP_F 0
#define AMP_BID 0
#include <hip/hip_runtime.h>
#include <stdint.h>

typedef unsigned u32;
typedef unsigned long long u64;
typedef unsigned short u16;
typedef u32 v4u __attribute__((ext_vector_type(4)));
typedef u32 v2u __attribute__((ext_vector_type(2)));
typedef float f4 __attribute__((ext_vector_type(4)));
typedef __bf16 bf8 __attribute__((ext_vector_type(8)));
typedef __bf16 bf2 __attribute__((ext_vector_type(2)));
typedef short s4 __attribute__((ext_vector_type(4)));
#define GAS __attribute__((address_space(1)))
#define RLX_AGENT __ATOMIC_RELAXED, __HIP_MEMORY_SCOPE_AGENT
#define SB() __builtin_amdgcn_sched_barrier(0)

#define QSLEEP 12
#define WS_BASE 0

constexpr int SK = 32768, WIN = 4096;
constexpr int AUX_NT = 2, AUX_SC1 = 16;
constexpr u32 RSRC3 = 0x00020000u;
constexpr u32 OFF_Q = 4096;
constexpr u32 OFF_P2 = OFF_Q + 2 * 8 * 4 * 512 * 8;
constexpr u32 OFF_ST = OFF_P2 + 2 * 32 * 16 * 128 * 8;
constexpr u32 OFF_P3 = OFF_ST + 2 * 32 * 16 * 8;
constexpr u32 WS_BYTES = OFF_P3 + 32 * 8 * 128 * 8;
constexpr u32 SPIN_CAP = 1u << 14;
constexpr float C2 = 0.08838834764831845f * 1.4426950408889634f;

template <int CTRL> __device__ __forceinline__ float dpp(float x) {
  return __builtin_bit_cast(float, __builtin_amdgcn_mov_dpp(__builtin_bit_cast(int, x), CTRL, 0xf, 0xf, true));
}
constexpr int XOR1 = 0xB1, XOR2 = 0x4E, XOR7 = 0x141, XOR8 = 0x128;
__device__ __forceinline__ float x16_sum(float x) { auto s = __builtin_amdgcn_permlane16_swap(__float_as_uint(x), __float_as_uint(x), false, false); return __uint_as_float(s[0]) + __uint_as_float(s[1]); }
__device__ __forceinline__ float x32_sum(float x) { auto s = __builtin_amdgcn_permlane32_swap(__float_as_uint(x), __float_as_uint(x), false, false); return __uint_as_float(s[0]) + __uint_as_float(s[1]); }
__device__ __forceinline__ float x16_max(float x) { auto s = __builtin_amdgcn_permlane16_swap(__float_as_uint(x), __float_as_uint(x), false, false); return fmaxf(__uint_as_float(s[0]), __uint_as_float(s[1])); }
__device__ __forceinline__ float x32_max(float x) { auto s = __builtin_amdgcn_permlane32_swap(__float_as_uint(x), __float_as_uint(x), false, false); return fmaxf(__uint_as_float(s[0]), __uint_as_float(s[1])); }
__device__ __forceinline__ u32 pk_bf16(float lo, float hi) { bf2 v = {(__bf16)lo, (__bf16)hi}; return __builtin_bit_cast(u32, v); }
__device__ __forceinline__ float bf_lo(u32 v) { return __uint_as_float(v << 16); }
__device__ __forceinline__ float bf_hi(u32 v) { return __uint_as_float(v & 0xffff0000u); }
__device__ __forceinline__ u32 enc(float f, u32 tl) { return ((__float_as_uint(f) + 0x80u) >> 8) | tl; }
__device__ __forceinline__ float dec(u32 lo) { return __uint_as_float(lo << 8); }
__device__ __forceinline__ u32 chk(u32 lo, u32 hi, u32 tl, u32 th) { return ((lo ^ tl) & 0xff000000u) | (hi ^ th); }
__device__ __forceinline__ u32 perm_lo(u32 a, u32 b) { return __builtin_amdgcn_perm(b, a, 0x05040100u); }
__device__ __forceinline__ u32 perm_hi(u32 a, u32 b) { return __builtin_amdgcn_perm(b, a, 0x07060302u); }

#define STAMP(i) do { SB(); asm volatile("s_memrealtime %0\n\ts_waitcnt lgkmcnt(0)" : "=s"(tst[i]) :: "memory"); SB(); } while (0)
#define STAMP_DECL u64 tst[13]; _Pragma("unroll") for (int i_ = 0; i_ < 13; ++i_) tst[i_] = 0
#define STAMP_FLUSH do { if (bid == AMP_BID && w == 0) { const u64 until = tst[12] + AMP_F * (tst[AMP_I] - tst[0]); for (int sp_ = 0; sp_ < 100000 && __builtin_amdgcn_s_memrealtime() < until; ++sp_) {} } } while (0)

constexpr int RED_H = 132, RED_W = 4 * RED_H;

__global__ void __launch_bounds__(1024) fused_decode(const u16* __restrict__ x, const u16* __restrict__ kc, const u16* __restrict__ vc,
                                                     const u16* __restrict__ wq, const u16* __restrict__ wo, u16* __restrict__ out,
                                                     unsigned char* __restrict__ ws
) {
  __shared__ __attribute__((aligned(16))) float lds[256 + 16 * RED_W + 128 + 16 * 128];
  u32* q_lds = (u32*)lds;
  float* red = lds + 256;
  float* stm = red + 16 * RED_W;
  float* red2 = stm + 128;
  const int bid = blockIdx.x, tid = threadIdx.x, lane = tid & 63, w = __builtin_amdgcn_readfirstlane(tid >> 6);
  const int kvh = bid & 7, r = bid >> 3, b = r & 1, s = r >> 1;
  STAMP_DECL;
  STAMP(0);
  const auto rws = __builtin_amdgcn_make_buffer_rsrc(ws, 0, WS_BYTES, RSRC3);
  const v2u ev = __builtin_amdgcn_raw_buffer_load_b64(rws, 0, 0, AUX_SC1);
  SB();
  const int l15 = lane & 15, g4 = lane >> 4;

  if (w == 0) {
    const int cb = r & 7, ks = r >> 3;
    const auto rq = __builtin_amdgcn_make_buffer_rsrc((void*)(wq + (size_t)ks * 128 * 4096 + kvh * 512 + cb * 64), 0, 128 * 8192, RSRC3);
    const auto rx = __builtin_amdgcn_make_buffer_rsrc((void*)x, 0, 2048, RSRC3);
    const int hh = (lane >> 3) & 1, c = lane & 7;
    v4u xa[2], wv[16];
    const u32 xoff = ((lane & 1) * 512 + ks * 128 + ((lane >> 1) & 1) * 32 + g4 * 8) * 2;
    xa[0] = __builtin_amdgcn_raw_buffer_load_b128(rx, xoff, 0, 0);
    xa[1] = __builtin_amdgcn_raw_buffer_load_b128(rx, xoff + 128, 0, 0);
    const u32 woff = (hh * 32 + g4 * 8) * 8192 + c * 16;
#pragma unroll
    for (int i = 0; i < 16; ++i) wv[i] = __builtin_amdgcn_raw_buffer_load_b128(rq, woff, ((i >> 3) * 64 + (i & 7)) * 8192, AUX_NT);
    SB();
    __builtin_amdgcn_s_barrier();
    SB();
    f4 acc[8];
#pragma unroll
    for (int e = 0; e < 8; ++e) acc[e] = f4{0.f, 0.f, 0.f, 0.f};
#pragma unroll
    for (int t = 0; t < 2; ++t)
#pragma unroll
      for (int e = 0; e < 8; ++e) {
        v4u bb;
#pragma unroll
        for (int m = 0; m < 4; ++m) {
          const u32 a0 = wv[8 * t + 2 * m][e >> 1], a1 = wv[8 * t + 2 * m + 1][e >> 1];
          bb[m] = (e & 1) ? perm_hi(a0, a1) : perm_lo(a0, a1);
        }
        acc[e] = __builtin_amdgcn_mfma_f32_16x16x32_bf16(__builtin_bit_cast(bf8, xa[t]), __builtin_bit_cast(bf8, bb), acc[e], 0, 0, 0);
      }
    const bool pb = lane >= 32, e1 = (lane >> 3) & 1, e2 = (lane >> 4) & 1;
    float v[8];
#pragma unroll
    for (int e = 0; e < 8; ++e) {
      const float lo = pb ? acc[e][1] : acc[e][0], hi = pb ? acc[e][3] : acc[e][2];
      const float t = hh ? hi : lo;
      v[e] = t + dpp<XOR8>(t);
    }
    const float a0 = e1 ? v[2] : v[0], a1 = e1 ? v[3] : v[1], a2 = e1 ? v[6] : v[4], a3 = e1 ? v[7] : v[5];
    const float p0 = e2 ? a2 : a0, p1 = e2 ? a3 : a1;
    v2u e0 = ev; asm volatile("" : "+v"(e0));
    const u64 T = (((u64)e0[1] << 32) | e0[0]) + 1;
    const u32 th = (u32)T, tl = ((u32)(T >> 32)) << 24;
    const v4u gr = {enc(p0, tl), th, enc(p1, tl), th};
    __builtin_amdgcn_raw_buffer_store_b128(gr, rws, OFF_Q + ((((lane >> 5) * 8 + kvh) * 4 + ks) * 512 + cb * 64 + (lane & 31) * 2) * 8, 0, AUX_SC1);
    SB();
    STAMP(1);
  } else {
    __builtin_amdgcn_s_barrier();
    SB();
  }
  const size_t kvbase = ((size_t)(b * 8 + kvh) * SK + (SK - WIN) + s * 256) * 128;
  const auto rk = __builtin_amdgcn_make_buffer_rsrc((void*)(kc + kvbase), 0, 65536, RSRC3);
  const auto rv = __builtin_amdgcn_make_buffer_rsrc((void*)(vc + kvbase), 0, 65536, RSRC3);
  v4u kf[4], vf[4];
#pragma unroll
  for (int i = 0; i < 4; ++i) kf[i] = __builtin_amdgcn_raw_buffer_load_b128(rk, l15 * 256 + g4 * 16 + i * 64, w * 4096, AUX_NT);
#pragma unroll
  for (int i = 0; i < 4; ++i) vf[i] = __builtin_amdgcn_raw_buffer_load_b128(rv, g4 * 1024 + l15 * 16 + i * 256, w * 4096, AUX_NT);
  SB();
  STAMP(2);
  v2u e1v = ev; asm volatile("" : "+v"(e1v));
  const u64 T = (((u64)e1v[1] << 32) | e1v[0]) + 1;
  const u32 th = __builtin_amdgcn_readfirstlane((u32)T), tl = __builtin_amdgcn_readfirstlane((u32)(T >> 32)) << 24;
  if (w == 0) {
    __builtin_amdgcn_s_sleep(QSLEEP);
    const u32 qoff = OFF_Q + (b * 8 + kvh) * 4 * 512 * 8 + lane * 16;
    v4u g[16];
    for (u32 spin = 0; spin < SPIN_CAP; ++spin) {
      asm volatile("" ::: "memory");
#pragma unroll
      for (int m = 0; m < 16; ++m) g[m] = __builtin_amdgcn_raw_buffer_load_b128(rws, qoff, m * 1024, AUX_SC1);
      u32 bad = 0;
#pragma unroll
      for (int m = 0; m < 16; ++m) bad |= chk(g[m][0], g[m][1], tl, th) | chk(g[m][2], g[m][3], tl, th);
      if (__all(bad == 0)) break;
      __builtin_amdgcn_s_sleep(2);
    }
    const int l5 = lane & 31, col = (lane >> 5) * 64 + (l5 & 7) * 8 + (l5 >> 3) * 2;
#pragma unroll
    for (int h = 0; h < 4; ++h) {
      const float q0 = (dec(g[h][0]) + dec(g[4 + h][0])) + (dec(g[8 + h][0]) + dec(g[12 + h][0]));
      const float q1 = (dec(g[h][2]) + dec(g[4 + h][2])) + (dec(g[8 + h][2]) + dec(g[12 + h][2]));
      q_lds[h * 64 + (col >> 1)] = pk_bf16(q0, q1);
    }
    STAMP(3);
  }
  __syncthreads();
  STAMP(4);
  const int h3 = kvh * 4 + (r >> 3), cb3 = r & 7;
  const auto ro = __builtin_amdgcn_make_buffer_rsrc((void*)(wo + (size_t)h3 * 128 * 512 + cb3 * 64), 0, 128 * 1024, RSRC3);
  const v4u wof = __builtin_amdgcn_raw_buffer_load_b128(ro, (lane & 7) * 8192 + (lane >> 3) * 16, ((w & 1) * 64 + (w >> 1)) * 1024, AUX_NT);
  SB();
  v4u qf[4];
#pragma unroll
  for (int i = 0; i < 4; ++i) qf[i] = *(const v4u*)(q_lds + (lane & 3) * 64 + i * 16 + g4 * 4);
  f4 sc = {0.f, 0.f, 0.f, 0.f};
#pragma unroll
  for (int i = 0; i < 4; ++i) sc = __builtin_amdgcn_mfma_f32_16x16x32_bf16(__builtin_bit_cast(bf8, kf[i]), __builtin_bit_cast(bf8, qf[i]), sc, 0, 0, 0);
  float mw = fmaxf(fmaxf(sc[0], sc[1]), fmaxf(sc[2], sc[3]));
  mw = x32_max(x16_max(mw)) * C2;
  const u32 p01 = pk_bf16(__builtin_amdgcn_exp2f(fmaf(sc[0], C2, -mw)), __builtin_amdgcn_exp2f(fmaf(sc[1], C2, -mw)));
  const u32 p23 = pk_bf16(__builtin_amdgcn_exp2f(fmaf(sc[2], C2, -mw)), __builtin_amdgcn_exp2f(fmaf(sc[3], C2, -mw)));
  float lw = (bf_lo(p01) + bf_hi(p01)) + (bf_lo(p23) + bf_hi(p23));
  lw = x32_sum(x16_sum(lw));
  const v2u pp = {p01, p23};
  STAMP(5);
  f4 oa[8];
#pragma unroll
  for (int e = 0; e < 8; ++e) {
    const v2u va = {(e & 1) ? perm_hi(vf[0][e >> 1], vf[1][e >> 1]) : perm_lo(vf[0][e >> 1], vf[1][e >> 1]),
                    (e & 1) ? perm_hi(vf[2][e >> 1], vf[3][e >> 1]) : perm_lo(vf[2][e >> 1], vf[3][e >> 1])};
    oa[e] = __builtin_amdgcn_mfma_f32_16x16x16bf16_1k(__builtin_bit_cast(s4, va), __builtin_bit_cast(s4, pp), f4{0.f, 0.f, 0.f, 0.f}, 0, 0, 0);
  }
  if (l15 < 4) {
#pragma unroll
    for (int e = 0; e < 8; ++e) *(f4*)(red + w * RED_W + l15 * RED_H + e * 16 + g4 * 4) = oa[e];
    if (g4 == 0) { stm[l15 * 16 + w] = mw; stm[64 + l15 * 16 + w] = lw; }
  }
  STAMP(6);
  __syncthreads();
  STAMP(7);
  {
    const int hd = w >> 2, pos = (w & 3) * 32 + (lane & 31), half = lane >> 5;
    f4 mm[4];
#pragma unroll
    for (int i = 0; i < 4; ++i) mm[i] = *(const f4*)(stm + hd * 16 + i * 4);
    const f4 l0 = *(const f4*)(stm + 64 + hd * 16 + half * 8), l1 = *(const f4*)(stm + 64 + hd * 16 + half * 8 + 4);
    float ov[8];
#pragma unroll
    for (int i = 0; i < 8; ++i) ov[i] = red[(half * 8 + i) * RED_W + hd * RED_H + pos];
    float M = fmaxf(fmaxf(fmaxf(mm[0][0], mm[0][1]), fmaxf(mm[0][2], mm[0][3])), fmaxf(fmaxf(mm[1][0], mm[1][1]), fmaxf(mm[1][2], mm[1][3])));
    M = fmaxf(M, fmaxf(fmaxf(fmaxf(mm[2][0], mm[2][1]), fmaxf(mm[2][2], mm[2][3])), fmaxf(fmaxf(mm[3][0], mm[3][1]), fmaxf(mm[3][2], mm[3][3]))));
    const f4 ma = half ? mm[2] : mm[0], mb = half ? mm[3] : mm[1];
    float O = 0.f, L = 0.f;
#pragma unroll
    for (int i = 0; i < 4; ++i) { const float a = __builtin_amdgcn_exp2f(ma[i] - M); O = fmaf(a, ov[i], O); L = fmaf(a, l0[i], L); }
#pragma unroll
    for (int i = 0; i < 4; ++i) { const float a = __builtin_amdgcn_exp2f(mb[i] - M); O = fmaf(a, ov[4 + i], O); L = fmaf(a, l1[i], L); }
    O = x32_sum(O); L = x32_sum(L);
    O *= __builtin_amdgcn_rcpf(L);
    const float On = dpp<XOR1>(O);
    const u32 rec = (b * 32 + kvh * 4 + hd) * 16 + s;
    if (lane < 32 && !(lane & 1)) {
      const v4u gr = {enc(O, tl), th, enc(On, tl), th};
      __builtin_amdgcn_raw_buffer_store_b128(gr, rws, OFF_P2 + (rec * 128 + pos) * 8, 0, AUX_SC1);
    }
    if ((w & 3) == 0 && lane == 32) {
      const v2u gr = {__float_as_uint(M + __builtin_amdgcn_logf(L)), th};
      __builtin_amdgcn_raw_buffer_store_b64(gr, rws, OFF_ST + rec * 8, 0, AUX_SC1);
    }
  }
  SB();
  __builtin_amdgcn_s_barrier();
  SB();
  STAMP(8);
  {
    const int k8 = lane >> 3, row = lane & 7;
    const u32 poff = OFF_P2 + ((h3 * 16 + 2 * k8) * 128 + w * 8 + row) * 8, soff = OFF_ST + (h3 * 16 + 2 * k8) * 8;
    v2u g[4]; v4u st[2];
    for (u32 spin = 0; spin < SPIN_CAP; ++spin) {
      asm volatile("" ::: "memory");
#pragma unroll
      for (int m = 0; m < 4; ++m) g[m] = __builtin_amdgcn_raw_buffer_load_b64(rws, poff, (m >> 1) * (32 * 16 * 128 * 8) + (m & 1) * 1024, AUX_SC1);
#pragma unroll
      for (int m = 0; m < 2; ++m) st[m] = __builtin_amdgcn_raw_buffer_load_b128(rws, soff, m * (32 * 16 * 8), AUX_SC1);
      u32 bad = (st[0][1] ^ th) | (st[0][3] ^ th) | (st[1][1] ^ th) | (st[1][3] ^ th);
#pragma unroll
      for (int m = 0; m < 4; ++m) bad |= chk(g[m][0], g[m][1], tl, th);
      if (__all(bad == 0)) break;
      __builtin_amdgcn_s_sleep(1);
    }
    STAMP(9);
    float ob[2];
#pragma unroll
    for (int bb = 0; bb < 2; ++bb) {
      const float s0 = __uint_as_float(st[bb][0]), s1 = __uint_as_float(st[bb][2]);
      float M = fmaxf(s0, s1); M = fmaxf(M, dpp<XOR8>(M)); M = x32_max(x16_max(M));
      const float w0 = __builtin_amdgcn_exp2f(s0 - M), w1 = __builtin_amdgcn_exp2f(s1 - M);
      float L = w0 + w1, O = fmaf(w0, dec(g[2 * bb][0]), w1 * dec(g[2 * bb + 1][0]));
      L += dpp<XOR8>(L); O += dpp<XOR8>(O);
      L = x32_sum(x16_sum(L)); O = x32_sum(x16_sum(O));
      ob[bb] = (float)(__bf16)(O * __builtin_amdgcn_rcpf(L));
    }
    float y[16];
#pragma unroll
    for (int i = 0; i < 4; ++i) {
      const float lo = bf_lo(wof[i]), hi = bf_hi(wof[i]);
      y[2 * i] = ob[0] * lo; y[2 * i + 1] = ob[0] * hi; y[8 + 2 * i] = ob[1] * lo; y[8 + 2 * i + 1] = ob[1] * hi;
    }
    const bool r4 = row & 4, r2 = row & 2, r1 = row & 1;
    float z[8];
#pragma unroll
    for (int i = 0; i < 8; ++i) { const float keep = r4 ? y[8 + i] : y[i], send = r4 ? y[i] : y[8 + i]; z[i] = keep + dpp<XOR7>(send); }
    float u[4];
#pragma unroll
    for (int i = 0; i < 4; ++i) { const float keep = r2 ? z[4 + i] : z[i], send = r2 ? z[i] : z[4 + i]; u[i] = keep + dpp<XOR2>(send); }
    float t2[2];
#pragma unroll
    for (int i = 0; i < 2; ++i) { const float keep = r1 ? u[2 + i] : u[i], send = r1 ? u[i] : u[2 + i]; t2[i] = keep + dpp<XOR1>(send); }
    *(float2*)(red2 + w * 128 + (row >> 2) * 64 + k8 * 8 + (row & 3) * 2) = make_float2(t2[0], t2[1]);
  }
  __syncthreads();
  STAMP(10);
  {
    const int v = w * 8 + (lane >> 3), part = lane & 7;
    float t = red2[(2 * part) * 128 + v] + red2[(2 * part + 1) * 128 + v];
    t += dpp<XOR1>(t); t += dpp<XOR2>(t); t += dpp<XOR7>(t);
    const float tn = dpp<XOR8>(t);
    if ((lane & 15) == 0) {
      const v4u gr = {enc(t, tl), th, enc(tn, tl), th};
      __builtin_amdgcn_raw_buffer_store_b128(gr, rws, OFF_P3 + ((h3 * 8 + cb3) * 128 + v) * 8, 0, AUX_SC1);
    }
  }
  SB();
  __builtin_amdgcn_s_barrier();
  SB();
  STAMP(11);
  if (w == 0) {
    const int cbf = bid >> 5, v0 = (bid & 31) * 4;
    const u32 off = OFF_P3 + (((lane >> 1) * 8 + cbf) * 128 + v0 + (lane & 1) * 2) * 8;
    v4u g;
    for (u32 spin = 0; spin < SPIN_CAP; ++spin) {
      asm volatile("" ::: "memory");
      g = __builtin_amdgcn_raw_buffer_load_b128(rws, off, 0, AUX_SC1);
      const u32 bad = chk(g[0], g[1], tl, th) | chk(g[2], g[3], tl, th);
      if (__all(bad == 0)) break;
      __builtin_amdgcn_s_sleep(1);
    }
    float a0 = dec(g[0]), a1 = dec(g[2]);
    a0 += dpp<XOR2>(a0); a1 += dpp<XOR2>(a1);
    a0 += dpp<0x124>(a0); a1 += dpp<0x124>(a1);
    a0 += dpp<XOR8>(a0); a1 += dpp<XOR8>(a1);
    a0 = x32_sum(x16_sum(a0)); a1 = x32_sum(x16_sum(a1));
    if (lane < 2) *(u32*)(out + (v0 >> 6) * 512 + cbf * 64 + (v0 & 63) + lane * 2) = pk_bf16(a0, a1);
    if (bid == 0 && lane == 0) __hip_atomic_store((GAS u64*)ws, T, RLX_AGENT);
    STAMP(12);
  }
  STAMP_FLUSH;
}

__global__ void k_null(int* p) { if (p == nullptr) *p = 0; }
__global__ void __launch_bounds__(1024) k_fat(int* p) { __shared__ int sm[10880]; if (p == nullptr) { asm volatile("v_mov_b32 v120, 0" ::: "v120"); sm[threadIdx.x] = 1; __syncthreads(); *p = sm[(threadIdx.x * 7) % 10880]; } }
__global__ void __launch_bounds__(1024) k_fatlds(int* p) { __shared__ int sm[10880]; if (p == nullptr) { sm[threadIdx.x] = 1; __syncthreads(); *p = sm[(threadIdx.x * 7) % 10880]; } }
__global__ void __launch_bounds__(1024) k_fatreg(int* p) { if (p == nullptr) { asm volatile("v_mov_b32 v120, 0" ::: "v120"); *p = 1; } }
extern "C" void kernel_launch(void* const* d_in, const int* in_sizes, int n_in, void* d_out, int out_size, void* d_ws, size_t ws_size,
                              hipStream_t stream) {
  fused_decode<<<256, 1024, 0, stream>>>((const u16*)d_in[0], (const u16*)d_in[1], (const u16*)d_in[2], (const u16*)d_in[3],
                                         (const u16*)d_in[4], (u16*)d_out, (unsigned char*)d_ws + WS_BASE);
  k_fatlds<<<256, 1024, 0, stream>>>((int*)d_ws);
}
```

```cpp
#include <hip/hip_runtime.h>
#include <stdint.h>
#pragma clang diagnostic ignored "-Wint-to-pointer-cast"

typedef unsigned u32;
typedef unsigned long long u64;
typedef unsigned short u16;
typedef u32 v4u __attribute__((ext_vector_type(4)));
typedef int v4i __attribute__((ext_vector_type(4)));
typedef u32 v2u __attribute__((ext_vector_type(2)));
typedef float f4 __attribute__((ext_vector_type(4)));
typedef __bf16 bf8 __attribute__((ext_vector_type(8)));
typedef __bf16 bf2 __attribute__((ext_vector_type(2)));
typedef short s4 __attribute__((ext_vector_type(4)));
#define GAS __attribute__((address_space(1)))
#define LAS __attribute__((address_space(3)))
#define RLX_AGENT __ATOMIC_RELAXED, __HIP_MEMORY_SCOPE_AGENT
#define SB() __builtin_amdgcn_sched_barrier(0)

constexpr int T_G = 300;
constexpr int D_2 = 25;
constexpr int D_3 = 14;
constexpr int D_K = 12;

constexpr int SK = 32768, WIN = 4096;
constexpr int AUX_SC1 = 16;
constexpr u32 RSRC3 = 0x00020000u;
constexpr u32 OFF_Q = 4096;
constexpr u32 OFF_P2 = OFF_Q + 2 * 8 * 4 * 256 * 8;
constexpr u32 OFF_P3 = OFF_P2 + 2 * 32 * 16 * 64 * 8;
constexpr u32 WS_BYTES = OFF_P3 + 64 * 4 * 128 * 8;
constexpr u32 SPIN_CAP = 1u << 18;
constexpr float C2 = 0.08838834764831845f * 1.4426950408889634f;
constexpr u32 L_K = 0, L_V = 65536, L_W = 131072, L_P = 147456, P_STR = 544, L_Q = L_P + 4 * P_STR, Q_STR = 272,
              L_M = L_Q + 4 * Q_STR, L_O = L_M + 128, O_STR = 144, L_X = L_O + 2 * O_STR, L_C = L_X + 512, L_END = L_C + 16;

template <int CTRL> __device__ __forceinline__ float dpp(float x) {
  return __builtin_bit_cast(float, __builtin_amdgcn_mov_dpp(__builtin_bit_cast(int, x), CTRL, 0xf, 0xf, true));
}
constexpr int XOR1 = 0xB1, XOR2 = 0x4E, ROR4 = 0x124, ROR8 = 0x128, BCAST = 0x150;
__device__ __forceinline__ float fmax3(float a, float b, float c) { float r; asm("v_max3_f32 %0, %1, %2, %3" : "=v"(r) : "v"(a), "v"(b), "v"(c)); return r; }
__device__ __forceinline__ float fmax2(float a, float b) { float r; asm("v_max_f32 %0, %1, %2" : "=v"(r) : "v"(a), "v"(b)); return r; }
#define MAX_DPP(X, MODE) asm("s_nop 1\n\tv_max_f32_dpp %0, %0, %0 " MODE " row_mask:0xf bank_mask:0xf bound_ctrl:1" : "+v"(X))
__device__ __forceinline__ float sw16_add(float a, float b) { auto s = __builtin_amdgcn_permlane16_swap(__float_as_uint(a), __float_as_uint(b), false, false); return __uint_as_float(s[0]) + __uint_as_float(s[1]); }
__device__ __forceinline__ float sw32_add(float a, float b) { auto s = __builtin_amdgcn_permlane32_swap(__float_as_uint(a), __float_as_uint(b), false, false); return __uint_as_float(s[0]) + __uint_as_float(s[1]); }
__device__ __forceinline__ float sw16_max(float a) { auto s = __builtin_amdgcn_permlane16_swap(__float_as_uint(a), __float_as_uint(a), false, false); return fmax2(__uint_as_float(s[0]), __uint_as_float(s[1])); }
__device__ __forceinline__ float sw32_max(float a) { auto s = __builtin_amdgcn_permlane32_swap(__float_as_uint(a), __float_as_uint(a), false, false); return fmax2(__uint_as_float(s[0]), __uint_as_float(s[1])); }
__device__ __forceinline__ u32 pk_bf16(float lo, float hi) { bf2 v = {(__bf16)lo, (__bf16)hi}; return __builtin_bit_cast(u32, v); }
__device__ __forceinline__ v2u enc24(float f0, float f1, u32 top) {
  const u32 r0 = __float_as_uint(f0) + 0x80u, r1 = __float_as_uint(f1) + 0x80u;
  return v2u{__builtin_amdgcn_perm(r1, r0, 0x05030201u), __builtin_amdgcn_perm(top, r1, 0x07060302u)};
}
__device__ __forceinline__ float dec24a(u32 lo) { return __uint_as_float(lo << 8); }
__device__ __forceinline__ float dec24b(u32 lo, u32 hi) { return __uint_as_float(__builtin_amdgcn_perm(hi, lo, 0x0504030cu)); }
__device__ __forceinline__ v2u enc20(float f0, float f1, u32 top) {
  const u32 r0 = __float_as_uint(f0) + 0x800u, r1 = __float_as_uint(f1) + 0x800u;
  return v2u{(r0 >> 12) | ((r1 >> 12) << 20), __builtin_amdgcn_perm(top, r1, 0x07060503u)};
}
__device__ __forceinline__ float dec20a(u32 lo) { return __uint_as_float(lo << 12); }
__device__ __forceinline__ float dec20b(u32 lo, u32 hi) { return __uint_as_float(__builtin_amdgcn_alignbit(hi, lo, 8) & 0xfffff000u); }
__device__ __forceinline__ v4i mkr(const void* p, u32 n) { const u64 a = (u64)p; return v4i{(int)(u32)a, (int)((u32)(a >> 32) & 0xffffu), (int)n, (int)RSRC3}; }
__device__ __forceinline__ v2u tr16(u32 a) { return __builtin_bit_cast(v2u, __builtin_amdgcn_ds_read_tr16_b64_v4i16((LAS s4*)a)); }
__device__ __forceinline__ v4u lds128(u32 a) { return *(LAS v4u*)a; }
__device__ __forceinline__ f4 mfma32(v4u a, v4u b, f4 c) { return __builtin_amdgcn_mfma_f32_16x16x32_bf16(__builtin_bit_cast(bf8, a), __builtin_bit_cast(bf8, b), c, 0, 0, 0); }
__device__ __forceinline__ v4u cat(v2u a, v2u b) { return v4u{a[0], a[1], b[0], b[1]}; }

#define DMA4(M0, V0, V1, V2, V3, RS, SOFF) asm volatile("s_mov_b32 m0, %0\n\ts_nop 0\n\t" \
    "buffer_load_dwordx4 %1, %5, %6 offen nt lds\n\tbuffer_load_dwordx4 %2, %5, %6 offen offset:1024 nt lds\n\t" \
    "buffer_load_dwordx4 %3, %5, %6 offen offset:2048 nt lds\n\tbuffer_load_dwordx4 %4, %5, %6 offen offset:3072 nt lds" \
    :: "s"(M0), "v"(V0), "v"(V1), "v"(V2), "v"(V3), "s"(RS), "s"(SOFF) : "memory")
#define DMA2(M0, V0, V1, RS, SOFF) asm volatile("s_mov_b32 m0, %0\n\ts_nop 0\n\tbuffer_load_dwordx4 %1, %3, %4 offen nt lds\n\t" \
    "buffer_load_dwordx4 %2, %3, %4 offen offset:1024 nt lds" :: "s"(M0), "v"(V0), "v"(V1), "s"(RS), "s"(SOFF) : "memory")
#define DMA1(M0, V0, RS) asm volatile("s_mov_b32 m0, %0\n\ts_nop 0\n\tbuffer_load_dwordx4 %1, %2, 0 offen lds" :: "s"(M0), "v"(V0), "s"(RS) : "memory")
#define WAITV(N) asm volatile("s_waitcnt vmcnt(%0)" :: "i"(N) : "memory")
#define BAR() do { SB(); asm volatile("s_waitcnt lgkmcnt(0)\n\ts_barrier" ::: "memory"); SB(); } while (0)
#define BARX() do { SB(); asm volatile("s_barrier" ::: "memory"); SB(); } while (0)
#define LD2X4(G0, G1, OFF, RS, S1) asm volatile("buffer_load_dwordx4 %0, %2, %3, 0 offen sc1\n\tbuffer_load_dwordx4 %1, %2, %3, %4 offen sc1\n\t" \
    "s_waitcnt vmcnt(0)" : "=&v"(G0), "=&v"(G1) : "v"(OFF), "s"(RS), "s"(S1) : "memory")
#define LD1X4(G0, OFF, RS) asm volatile("buffer_load_dwordx4 %0, %1, %2, 0 offen sc1\n\ts_waitcnt vmcnt(0)" : "=&v"(G0) : "v"(OFF), "s"(RS) : "memory")
#define GATHER(LOAD, BAD) do { LOAD; if (__builtin_expect(__any(BAD), 0)) { _Pragma("unroll 1") for (u32 spin_ = 0; spin_ < SPIN_CAP; ++spin_) { \
    __builtin_amdgcn_s_sleep(2); LOAD; if (!__any(BAD)) break; } } } while (0)

__global__ void __launch_bounds__(1024, 4) fused_decode(const u16* x, const u16* kc, const u16* vc, const u16* wq, const u16* wo, u16* out,
                                                        unsigned char* ws, u64 pad) {
  __shared__ __attribute__((aligned(16))) unsigned char smem[L_END];
  const u32 lb = (u32)(size_t)smem;
  const int bid = blockIdx.x, tid = threadIdx.x, lane = tid & 63, w = __builtin_amdgcn_readfirstlane(tid >> 6);
  const int kvh = bid & 7, r = bid >> 3, b = r & 1, s = r >> 1;
#define KA_WAIT() asm volatile("s_waitcnt lgkmcnt(0)" :: "s"(x), "s"(kc), "s"(vc), "s"(wq), "s"(wo), "s"(out), "s"(ws), "s"(pad) : "memory")
  const int l15 = lane & 15, g4 = lane >> 4, q4 = l15 >> 2, p4 = lane & 3;
  const int cb = r & 7, ks = r >> 3;
  if (w < 4) {
    if (tid == 0) *(LAS u32*)(lb + L_C) = 0u;
    u32 vq0 = ((lane >> 3) << 13) | (((lane ^ ((lane >> 3) & 2)) & 7) << 4), vq1 = (vq0 ^ 64u) + 64512u, vq2 = vq0 + 2 * 64512u, vq3 = vq1 + 2 * 64512u;
    u32 qo = (ks * 128 + w * 32) * 8192 + kvh * 1024 + cb * 128, vx = (g4 * 512 + ks * 128 + l15 * 8) * 2, lw = lb + L_W + w * 4096;
    asm volatile("" : "+v"(vq0), "+v"(vq1), "+v"(vq2), "+v"(vq3), "+v"(vx));
    asm volatile("" : "+s"(qo), "+s"(lw));
    KA_WAIT();
    if (tid < 32) DMA1(lb + L_X, vx, mkr(x, 2048));
    DMA4(lw, vq0, vq1, vq2, vq3, mkr(wq, 4u << 20), qo);
  }
  u32 vo0 = (lane ^ ((lane >> 2) & 12)) << 4, vo1 = vo0 ^ 16u, vo2 = vo0 ^ 32u, vo3 = vo0 ^ 48u;
  u32 lo = (w & 4) * 8192 + (w & 3) * 4096;
  u32 kvo = ((b * 8 + kvh) * SK + (SK - WIN) + s * 256) * 256 + lo, ldst = lb + (w & 8) * (L_V / 8) + lo;
  asm volatile("" : "+v"(vo0), "+v"(vo1), "+v"(vo2), "+v"(vo3));
  asm volatile("" : "+s"(kvo), "+s"(lo), "+s"(ldst));
  KA_WAIT();
  const u64 wsa = (u64)ws;
  u64 E;
  const v4i rkv = mkr(w < 8 ? kc : vc, 128u << 20);
  u64 tgo;
  asm volatile("s_load_dwordx2 %0, %2, 0x0\n\ts_barrier\n\ts_memrealtime %1\n\ts_sleep %c12\n\ts_mov_b32 m0, %3\n\ts_nop 0\n\t"
               "buffer_load_dwordx4 %5, %9, %10 offen nt lds\n\tbuffer_load_dwordx4 %6, %9, %10 offen offset:1024 nt lds\n\t"
               "buffer_load_dwordx4 %7, %9, %10 offen offset:2048 nt lds\n\tbuffer_load_dwordx4 %8, %9, %10 offen offset:3072 nt lds\n\t"
               "s_mov_b32 m0, %4\n\ts_nop 0\n\t"
               "buffer_load_dwordx4 %5, %9, %11 offen nt lds\n\tbuffer_load_dwordx4 %6, %9, %11 offen offset:1024 nt lds\n\t"
               "buffer_load_dwordx4 %7, %9, %11 offen offset:2048 nt lds\n\tbuffer_load_dwordx4 %8, %9, %11 offen offset:3072 nt lds\n\t"
               "s_waitcnt lgkmcnt(0)"
               : "=&s"(E), "=&s"(tgo) : "s"(wsa), "s"(ldst), "s"(ldst + 16384), "v"(vo0), "v"(vo1), "v"(vo2), "v"(vo3), "s"(rkv), "s"(kvo), "s"(kvo + 16384), "i"(D_K) : "memory");
  const auto rws = __builtin_amdgcn_make_buffer_rsrc((void*)wsa, 0, WS_BYTES, RSRC3);
  const v4i rw = mkr(ws, WS_BYTES);
  const int rb = r >> 2, cb3 = r & 3;
  const f4 z = {0.f, 0.f, 0.f, 0.f};

  if (w < 4) {
    u32 ba = lb + L_W + (8 * g4 + q4) * 128 + (((((q4 >> 1) | ((g4 & 1) << 1)) ^ w) * 2 + (p4 >> 1)) << 4) + (p4 & 1) * 8;
    u32 xa = lb + L_X + (lane & 1) * 256 + g4 * 16;
    u32 qst = OFF_Q + (((g4 * 8 + kvh) * 4 + ks) * 512 + cb * 64 + w * 16 + l15) * 4;
    asm volatile("s_waitcnt vmcnt(8)" : "+v"(qst), "+v"(ba), "+v"(xa) :: "memory");
    asm volatile("ds_add_u32 %0, %1\n\ts_waitcnt lgkmcnt(0)" : : "v"(lb + L_C), "v"(1u) : "memory");
#pragma unroll 1
    for (u32 i = 0; i < (1u << 22) && *(volatile LAS u32*)(lb + L_C) < 256u; ++i) {}
    asm volatile("" ::: "memory");
    const v4u x0 = lds128(xa), x1 = lds128(xa + 64), x2 = lds128(xa + 128), x3 = lds128(xa + 192);
    const v4u b0 = cat(tr16(ba), tr16(ba + 512)), b1 = cat(tr16(ba + 4096), tr16(ba + 4608)), b2 = cat(tr16(ba + 8192), tr16(ba + 8704)), b3 = cat(tr16(ba + 12288), tr16(ba + 12800));
    SB();
    const f4 a0 = mfma32(x1, b1, mfma32(x0, b0, z)) + mfma32(x3, b3, mfma32(x2, b2, z));
    const float qv = g4 ? a0[1] : a0[0];
    const v2u qg = enc24(qv, dpp<XOR1>(qv), (u32)(E + 1) << 16);
    if ((lane & 33) == 0) __builtin_amdgcn_raw_buffer_store_b64(qg, rws, qst, 0, AUX_SC1);
  }
  if (w < 8) {
    if (w < 4) WAITV(1); else WAITV(0);
    const u32 tg = (u32)(E + 1) << 8;
    v4u kf[8];
    {
      const u32 ka_ = lb + L_K + lo + l15 * 256, sw = ((l15 & 3) << 2) | (l15 >> 2);
#pragma unroll
      for (int i = 0; i < 4; ++i) { kf[i] = lds128(ka_ + (((4 * i + g4) ^ sw) << 4)); kf[4 + i] = lds128(ka_ + 16384 + (((4 * i + g4) ^ sw) << 4)); }
    }
    u32 qa = lb + L_Q + p4 * Q_STR + g4 * 16, mw_a = lb + L_M + p4 * 32 + w * 4, pw = lb + L_P + p4 * P_STR + w * 64 + g4 * 8;
    const u32 tsw = (((2 * w + (p4 >> 1)) ^ ((q4 << 2) | ((2 * g4) & 3))) << 4) + (p4 & 1) * 8;
    u32 t0 = lb + L_V + ((g4 >> 1) * 64 + (g4 & 1) * 8 + q4) * 256 + tsw, t1 = (t0 ^ 16u) + 1024;
    u32 pa = lb + L_P + q4 * P_STR + p4 * 64 + g4 * 16, ma = lb + L_M + g4 * 32 + (lane & 7) * 4;
    u32 pst = OFF_P2 + ((b * 8 + kvh) * 16 + s) * 2048 + w * 256 + lane * 4, lsh = 4 * (l15 & 6);
    v4u ones = {0x3f803f80u, 0x3f803f80u, 0x3f803f80u, 0x3f803f80u};
    asm volatile("" : "+v"(kf[0]), "+v"(kf[1]), "+v"(kf[2]), "+v"(kf[3]), "+v"(kf[4]), "+v"(kf[5]), "+v"(kf[6]), "+v"(kf[7]),
                      "+v"(qa), "+v"(mw_a), "+v"(pw), "+v"(t0), "+v"(t1), "+v"(pa), "+v"(ma), "+v"(pst), "+v"(lsh), "+v"(ones));
    if (w < 4) {
      const u32 qoff = OFF_Q + (((b * 8 + kvh) * 4 + (lane >> 5)) * 256 + w * 64) * 8 + (lane & 31) * 16;
#pragma unroll 1
      for (int i = 0; i < 1000 && __builtin_amdgcn_s_memrealtime() < tgo + T_G; ++i) __builtin_amdgcn_s_sleep(1);
      v4u g0, g1;
      GATHER(LD2X4(g0, g1, qoff, rw, 4096), (((g0[1] ^ (tg << 8)) | (g0[3] ^ (tg << 8)) | (g1[1] ^ (tg << 8)) | (g1[3] ^ (tg << 8))) >> 16) != 0);
      const float qa_ = sw32_add(dec24a(g0[0]) + dec24a(g1[0]), dec24a(g0[2]) + dec24a(g1[2]));
      const float qb_ = sw32_add(dec24b(g0[0], g0[1]) + dec24b(g1[0], g1[1]), dec24b(g0[2], g0[3]) + dec24b(g1[2], g1[3]));
      *(LAS u32*)(lb + L_Q + w * Q_STR + (lane & 31) * 8 + (lane >> 5) * 4) = pk_bf16(qa_, qb_);
    }
    BAR();
#define VFRAG(t) cat(tr16(t0 + ((t) >> 2) * 32768 + ((t) & 3) * 4096), tr16(t1 + ((t) >> 2) * 32768 + ((t) & 3) * 4096))
    const v4u q0 = lds128(qa), q1 = lds128(qa + 64), q2 = lds128(qa + 128), q3 = lds128(qa + 192);
    v4u vf[8];
#pragma unroll
    for (int t = 0; t < 4; ++t) vf[t] = VFRAG(t);
    SB();
    const f4 sa = mfma32(kf[3], q3, mfma32(kf[2], q2, mfma32(kf[1], q1, mfma32(kf[0], q0, z)))) * C2;
    const f4 sb = mfma32(kf[7], q3, mfma32(kf[6], q2, mfma32(kf[5], q1, mfma32(kf[4], q0, z)))) * C2;
    const float mw = sw32_max(sw16_max(fmax3(fmax3(sa[0], sa[1], sa[2]), fmax3(sa[3], sb[0], sb[1]), fmax2(sb[2], sb[3]))));
    *(LAS v2u*)pw = v2u{pk_bf16(__builtin_amdgcn_exp2f(sa[0] - mw), __builtin_amdgcn_exp2f(sa[1] - mw)),
                        pk_bf16(__builtin_amdgcn_exp2f(sa[2] - mw), __builtin_amdgcn_exp2f(sa[3] - mw))};
    *(LAS v2u*)(pw + 32) = v2u{pk_bf16(__builtin_amdgcn_exp2f(sb[0] - mw), __builtin_amdgcn_exp2f(sb[1] - mw)),
                               pk_bf16(__builtin_amdgcn_exp2f(sb[2] - mw), __builtin_amdgcn_exp2f(sb[3] - mw))};
    *(LAS float*)mw_a = mw;
    BAR();
    const float mt = *(const LAS float*)ma;
    const v4u pf0 = lds128(pa), pf1 = lds128(pa + 256);
#pragma unroll
    for (int t = 4; t < 8; ++t) vf[t] = VFRAG(t);
    SB();
    const f4 l0 = mfma32(pf0, ones, z), l1 = mfma32(pf1, ones, z);
    const f4 d0 = mfma32(pf0, vf[0], z), d1 = mfma32(pf0, vf[1], z), d2 = mfma32(pf0, vf[2], z), d3 = mfma32(pf0, vf[3], z);
    const f4 d4 = mfma32(pf1, vf[4], z), d5 = mfma32(pf1, vf[5], z), d6 = mfma32(pf1, vf[6], z), d7 = mfma32(pf1, vf[7], z);
    float M = mt;
    MAX_DPP(M, "quad_perm:[1,0,3,2]"); MAX_DPP(M, "quad_perm:[2,3,0,1]"); MAX_DPP(M, "row_half_mirror");
    const float al = __builtin_amdgcn_exp2f(mt - M);
    const float a0 = dpp<BCAST>(al), a1 = dpp<BCAST + 1>(al), a2 = dpp<BCAST + 2>(al), a3 = dpp<BCAST + 3>(al),
                a4 = dpp<BCAST + 4>(al), a5 = dpp<BCAST + 5>(al), a6 = dpp<BCAST + 6>(al), a7 = dpp<BCAST + 7>(al);
    const float lsum = (fmaf(a0, l0[0], a1 * l0[1]) + fmaf(a2, l0[2], a3 * l0[3])) + (fmaf(a4, l1[0], a5 * l1[1]) + fmaf(a6, l1[2], a7 * l1[3]));
    const float rl = __builtin_amdgcn_rcpf(lsum);
    u32 lb8 = (((__float_as_uint(M + __builtin_amdgcn_logf(lsum)) >> lsh) & 0xffu) << 8) | (tg << 8);
    asm volatile("" : "+v"(lb8));
    const float o = ((fmaf(a0, d0[0], a1 * d1[1]) + fmaf(a2, d2[2], a3 * d3[3])) + (fmaf(a4, d4[0], a5 * d5[1]) + fmaf(a6, d6[2], a7 * d7[3]))) * rl;
    const v2u og = enc20(o, dpp<XOR1>(o), lb8);
    if (!(lane & 1)) __builtin_amdgcn_raw_buffer_store_b64(og, rws, pst, 0, AUX_SC1);
    BARX();
    u32 oa = lb + L_O + (q4 & 1) * O_STR + g4 * 16, yst = OFF_P3 + ((kvh * 8 + rb) * 4 + cb3) * 1024 + w * 128 + lane * 4;
    t0 = lb + L_W + (8 * g4 + q4) * 256 + tsw; t1 = (t0 ^ 16u) + 1024;
    asm volatile("" : "+v"(oa), "+v"(yst), "+v"(t0), "+v"(t1));
    BARX();
    const v4u oa0 = lds128(oa), oa1 = lds128(oa + 64), b0 = cat(tr16(t0), tr16(t1)), b1 = cat(tr16(t0 + 8192), tr16(t1 + 8192));
    SB();
    const float y = mfma32(oa0, b0, z)[0] + mfma32(oa1, b1, z)[0];
    const v2u yg = enc20(y, dpp<XOR1>(y), tg);
    if ((lane & 33) == 0) __builtin_amdgcn_raw_buffer_store_b64(yg, rws, yst, 0, AUX_SC1);
    BARX();
  } else {
    u32 tg = (u32)(E + 1) << 8;
    u32 wv0 = g4 * 1024 + ((l15 ^ (g4 << 2) ^ ((2 * w) & 3)) << 4), wv1 = (wv0 ^ 16u) + 3072u;
    u32 poff = OFF_P2 + ((((lane >> 1) & 1) * 8 + kvh) * 16 + (lane >> 2)) * 2048 + ((rb & 1) * 4 + ((w - 8) >> 1)) * 256 + (rb >> 1) * 64 + (w & 1) * 32 + (lane & 1) * 16;
    u32 ow = lb + L_O + ((lane >> 1) & 1) * O_STR + ((w - 8) * 8 + (lane & 1) * 4 + (lane >> 5) * 2 + (g4 & 1)) * 2;
    u32 sel = (lane & 1) ? 0x01000504u : 0x05040100u;
    const int cbf = kvh >> 1, v0 = (r >> 4) * 128 + ((kvh & 1) * 4 + (r & 3)) * 16 + ((r >> 2) & 3) * 4;
    u32 off = OFF_P3 + (lane * 4 + cbf) * 1024 + ((v0 >> 4) & 7) * 128 + (v0 >> 7) * 64 + (v0 & 15) * 4;
    u32 ooff = ((v0 >> 7) * 512 + cbf * 128 + (v0 & 127) + g4) * 2;
    asm volatile("" : "+v"(ow), "+v"(poff), "+v"(sel), "+v"(wv0), "+v"(wv1), "+v"(off), "+v"(ooff));
    if (w < 12) WAITV(0);
    BARX();
    WAITV(0);
    BARX();
    DMA2(lb + L_W + (w - 8) * 2048, wv0, wv1, mkr(wo, 4u << 20), (kvh * 512 + rb * 64 + (w - 8) * 8) * 1024 + cb3 * 256);
    BARX();
    __builtin_amdgcn_s_sleep(D_2);
    v4u g0;
    GATHER(LD1X4(g0, poff, rw), (((g0[1] ^ (tg << 8)) | (g0[3] ^ (tg << 8))) >> 16) != 0);
    {
      const u32 h0 = __builtin_amdgcn_perm(g0[3], g0[1], 0x05010501u);
      const float s0 = __uint_as_float(__builtin_amdgcn_perm(__builtin_amdgcn_mov_dpp(h0, 0xB1, 0xf, 0xf, true), h0, sel));
      float m = s0;
      MAX_DPP(m, "row_ror:4"); MAX_DPP(m, "row_ror:8");
      m = sw32_max(sw16_max(m));
      const float w0 = __builtin_amdgcn_exp2f(s0 - m);
      const float u0 = sw32_add(w0 * dec20a(g0[0]), w0 * dec20a(g0[2])), u1 = sw32_add(w0 * dec20b(g0[0], g0[1]), w0 * dec20b(g0[2], g0[3]));
      float ws_ = sw32_add(w0, w0);
      float t = sw16_add(u0, u1);
      ws_ = sw16_add(ws_, ws_);
      t += dpp<ROR4>(t); ws_ += dpp<ROR4>(ws_);
      t += dpp<ROR8>(t); ws_ += dpp<ROR8>(ws_);
      *(LAS __bf16*)ow = (__bf16)(t * __builtin_amdgcn_rcpf(ws_));
    }
    BAR();
    BARX();
    if (w == 8) {
      __builtin_amdgcn_s_sleep(D_3);
      GATHER(LD1X4(g0, off, rw), (((g0[1] ^ tg) | (g0[3] ^ tg)) >> 8) != 0);
      float a = sw16_add(sw32_add(dec20a(g0[0]), dec20a(g0[2])), sw32_add(dec20b(g0[0], g0[1]), dec20b(g0[2], g0[3])));
      a += dpp<ROR8>(a); a += dpp<ROR4>(a); a += dpp<XOR2>(a); a += dpp<XOR1>(a);
      if (l15 == 0) *(GAS __bf16*)((u64)out + ooff) = (__bf16)a;
      if (bid == 0 && lane == 0) __hip_atomic_store((GAS u64*)wsa, E + 1, RLX_AGENT);
    }
  }
}

extern "C" void kernel_launch(void* const* d_in, const int* in_sizes, int n_in, void* d_out, int out_size, void* d_ws, size_t ws_size,
                              hipStream_t stream) {
  fused_decode<<<256, 1024, 0, stream>>>((const u16*)d_in[0], (const u16*)d_in[1], (const u16*)d_in[2], (const u16*)d_in[3],
                                         (const u16*)d_in[4], (u16*)d_out, (unsigned char*)d_ws, 0ull);
}
```
